```python
import torch

def _softmax(x, axis=-1):
    return torch.softmax(x, dim=axis)
def _gelu(x):
    return torch.nn.functional.gelu(x, approximate="tanh")
def _silu(x):
    return torch.nn.functional.silu(x)
def _sigmoid(x):
    return torch.sigmoid(x)
def _layernorm(x, axis=-1, eps=1e-5):
    m = x.mean(dim=axis, keepdim=True)
    v = x.var(dim=axis, keepdim=True, unbiased=False)
    return (x - m) / torch.sqrt(v + eps)
def _scatter_add(tgt, idx, src, axis=0):
    return tgt.clone().index_add_(axis, idx, src)
def _segment_sum(src, seg, n):
    out = torch.zeros((n,) + src.shape[1:], dtype=src.dtype)
    return out.index_add_(0, seg, src)
def _pad(x, pw):
    flat = tuple(v for pair in reversed(pw) for v in pair)
    return torch.nn.functional.pad(x, flat)

kernel_name = 'recurrent_scan__ssm_conv_s4d__geglu_ffn__prompt'

def setup_inputs():

    _w = (lambda a: (lambda t: t.to(torch.bfloat16) if t.is_floating_point() else t)(torch.as_tensor(a)))
    def _A_setup():
        Bsz, L, D, N = 1, 2048, 640, 128
        return {
            "u": _w((lambda *s: torch.randn(*s, dtype=torch.bfloat16) * 0.05)(Bsz, L, D)),
            "log_A_real": _w((lambda *s: torch.randn(*s, dtype=torch.bfloat16) * 3.2)(D, N) * 0.5),
            "A_imag": _w((lambda *s: torch.randn(*s, dtype=torch.bfloat16) * 0.05)(D, N) * 3.14),
            "C_real": _w((lambda *s: torch.randn(*s, dtype=torch.bfloat16) * 0.1)(D, N) / N ** 0.5),
            "C_imag": _w((lambda *s: torch.randn(*s, dtype=torch.bfloat16) * 0.1)(D, N) / N ** 0.5),
            "log_dt": _w((lambda *s: torch.randn(*s, dtype=torch.bfloat16) * 0.05)(D) * 0.5 - 4.0),
            "D_skip": _w((lambda *s: torch.randn(*s, dtype=torch.bfloat16) * 0.05)(D)),
        }
    _A_in = _A_setup()

    def _B_setup():
        B, S, D, F = 1, 2048, 640, 1728
        return {
            "x": _w((lambda *s: torch.randn(*s, dtype=torch.bfloat16) * 0.05)(B, S, D)),
            "w_gate": _w((lambda *s: torch.randn(*s, dtype=torch.bfloat16) * 0.05)(D, F)),
            "w_up": _w((lambda *s: torch.randn(*s, dtype=torch.bfloat16) * 0.05)(D, F)),
            "w_down": _w((lambda *s: torch.randn(*s, dtype=torch.bfloat16) * 0.05)(F, D)),
        }
    _B_in = _B_setup()

    return {
        "A_u": _A_in["u"],
        "A_log_A_real": _A_in["log_A_real"],
        "A_A_imag": _A_in["A_imag"],
        "A_C_real": _A_in["C_real"],
        "A_C_imag": _A_in["C_imag"],
        "A_log_dt": _A_in["log_dt"],
        "A_D_skip": _A_in["D_skip"],
        "B_x": _B_in["x"],
        "B_w_gate": _B_in["w_gate"],
        "B_w_up": _B_in["w_up"],
        "B_w_down": _B_in["w_down"],
    }

def reference(A_u, A_log_A_real, A_A_imag, A_C_real, A_C_imag, A_log_dt, A_D_skip, B_x, B_w_gate, B_w_up, B_w_down):
    def _A_ref(u, log_A_real, A_imag, C_real, C_imag, log_dt, D_skip):
        L = u.shape[1]
        dt = torch.exp(log_dt)
        A = -torch.exp(log_A_real) + 1j * A_imag
        C = (C_real + 1j * C_imag) * dt[:, None]
        pos = (lambda x, dt: x.to(dt))(torch.arange(L), log_dt.dtype)
        P = torch.exp((A * dt[:, None])[:, :, None] * pos[None, None, :])
        K = 2.0 * torch.einsum("dn,dnl->dl", C, P).real
        L2 = 2 * L
        u_t = (lambda x, *d: x.permute(*d))(u, 0, 2, 1)
        Kf = (lambda x, *a, **k: torch.fft.rfft(x.float(), *a, **k))(K, L2)
        Uf = (lambda x, *a, **k: torch.fft.rfft(x.float(), *a, **k))(u_t, L2)
        y = (lambda x, *a, **k: torch.fft.irfft(x, *a, **k).to(torch.bfloat16))(Uf * Kf[None, :, :], L2)[:, :, :L]
        y = (lambda x, *d: x.permute(*d))(y, 0, 2, 1)
        return y + u * D_skip[None, None, :]

    def _B_ref(x, w_gate, w_up, w_down):
        return (_gelu(x @ w_gate) * (x @ w_up)) @ w_down

    x = A_u
    xn = _layernorm(x, axis=-1)
    s = _A_ref(xn, A_log_A_real, A_A_imag, A_C_real, A_C_imag, A_log_dt, A_D_skip)
    g = _silu(_B_ref(xn, B_w_gate, B_w_up, B_w_down))
    return x + s * g
```

```cpp
#include <hip/hip_runtime.h>
#include <hip/hip_bf16.h>
#include <stdint.h>
#include <type_traits>
#include <utility>

#define DI __device__ __forceinline__
#define LAS __attribute__((address_space(3)))
#define GAS __attribute__((address_space(1)))
typedef short s16x8 __attribute__((ext_vector_type(8)));
typedef short s16x4 __attribute__((ext_vector_type(4)));
typedef __bf16 bf16x8 __attribute__((ext_vector_type(8)));
typedef __bf16 bf16x2 __attribute__((ext_vector_type(2)));
typedef _Float16 f16x8 __attribute__((ext_vector_type(8)));
typedef _Float16 f16x2 __attribute__((ext_vector_type(2)));
typedef float f32x16 __attribute__((ext_vector_type(16)));
typedef float f32x4 __attribute__((ext_vector_type(4)));
typedef float f32x2 __attribute__((ext_vector_type(2)));
typedef unsigned u32x4 __attribute__((ext_vector_type(4)));
typedef unsigned u32x2 __attribute__((ext_vector_type(2)));
typedef LAS char* lds_ptr;
typedef const unsigned short* cu16p;
typedef __amdgpu_buffer_rsrc_t srd_t;

constexpr int L_ = 2048, D_ = 640, N_ = 128, F_ = 1728;
constexpr unsigned WS_SYNC = 0;
constexpr unsigned WS_STATS = 16384;
constexpr unsigned WS_KINV = 32768;
constexpr unsigned WS_H = 1u << 20;
constexpr unsigned WS_UT = 9u << 20;
constexpr unsigned WS_KR = 12u << 20;
constexpr unsigned WS_ST = 15u << 20;
constexpr unsigned WS_WD = 18u << 20;
constexpr unsigned WS_END = 21u << 20;
constexpr size_t WS_STAMP = (size_t)32 << 20;
constexpr int KR_STRIDE = 2080;
constexpr int SY_E = 0, SY_ACK = 64, SY_LN = 320, SY_WD = 480, SY_K = 640, SY_S = 1280, SY_H = 1920;
constexpr int SC1 = 16;
constexpr int LDS_BYTES = 163840;

#define STAMP_DECL do {} while (0)
#define STAMP(i) do {} while (0)
#define STAMP_FLUSH(ws, wv) do {} while (0)
#define TOUCH 0
#define NOCS 0
#define G2STAMP(i) STAMP(i)
#define NOHELP 0
#define NKG 2
#define NOCONV 0
#define WLATE 0
#define SB() __builtin_amdgcn_sched_barrier(0)

DI float bf2f(unsigned short v) { return __uint_as_float((unsigned)v << 16); }
DI float bflo(unsigned v) { return __uint_as_float(v << 16); }
DI float bfhi(unsigned v) { return __uint_as_float(v & 0xffff0000u); }
DI float rbf(float x) { return (float)(__bf16)x; }
DI unsigned pk_bf16(float lo, float hi) { bf16x2 v = {(__bf16)lo, (__bf16)hi}; return __builtin_bit_cast(unsigned, v); }
DI unsigned pk_f16(float lo, float hi) { f16x2 v = {(_Float16)lo, (_Float16)hi}; return __builtin_bit_cast(unsigned, v); }
DI s16x4 tr_read(lds_ptr p) { return __builtin_amdgcn_ds_read_tr16_b64_v4i16((LAS s16x4*)p); }
DI srd_t mk_srd(const void* p, unsigned bytes) { return __builtin_amdgcn_make_buffer_rsrc((void*)p, (short)0, (int)bytes, 0x00020000); }
DI void dma16(unsigned ldsaddr, unsigned voff, srd_t srd, unsigned soff) {
    asm volatile("s_mov_b32 m0, %0\n\ts_nop 0\n\tbuffer_load_dwordx4 %1, %2, %3 offen lds" :: "s"(ldsaddr), "v"(voff), "s"(srd), "s"(soff) : "memory");
}
DI void dma16_dev(unsigned ldsaddr, unsigned voff, srd_t srd, unsigned soff) {
    asm volatile("s_mov_b32 m0, %0\n\ts_nop 0\n\tbuffer_load_dwordx4 %1, %2, %3 offen sc1 lds" :: "s"(ldsaddr), "v"(voff), "s"(srd), "s"(soff) : "memory");
}
DI void hload(u32x4& dst, unsigned vo, srd_t srd, unsigned so) { asm volatile("buffer_load_dwordx4 %0, %1, %2, %3 offen sc1" : "=v"(dst) : "v"(vo), "s"(srd), "s"(so) : "memory"); }
DI u32x4 ld16_dev(srd_t s, unsigned vo, unsigned so) { return __builtin_amdgcn_raw_buffer_load_b128(s, vo, so, SC1); }
DI u32x2 ld8_dev(srd_t s, unsigned vo, unsigned so) { return __builtin_amdgcn_raw_buffer_load_b64(s, vo, so, SC1); }
DI void st16_dev(u32x4 v, srd_t s, unsigned vo, unsigned so) {
    asm volatile("buffer_store_dwordx4 %0, %1, %2, %3 offen sc1\n\ts_nop 1" :: "v"(v), "v"(vo), "s"(s), "s"(so)); }
DI void st8_dev(u32x2 v, srd_t s, unsigned vo, unsigned so) { __builtin_amdgcn_raw_buffer_store_b64(v, s, vo, so, SC1); }
#define WAIT_BAR(N) asm volatile("s_waitcnt vmcnt(" #N ") lgkmcnt(0)\n\ts_barrier" ::: "memory")
#define LBAR() asm volatile("s_waitcnt lgkmcnt(0)\n\ts_barrier" ::: "memory")
#define WAIT_VM(N) asm volatile("s_waitcnt vmcnt(" #N ")" ::: "memory")
#define WAIT_LG() asm volatile("s_waitcnt lgkmcnt(0)" ::: "memory")
DI unsigned sy_ld(GAS unsigned* p) { return __hip_atomic_load(p, __ATOMIC_RELAXED, __HIP_MEMORY_SCOPE_AGENT); }
DI void sy_st(GAS unsigned* p, unsigned v) { __hip_atomic_store(p, v, __ATOMIC_RELAXED, __HIP_MEMORY_SCOPE_AGENT); }
template <int K> DI void poll_all(GAS unsigned* p, int n, unsigned tag, int lane) {
    for (int it = 0; it < (1 << 18); ++it) {
        bool ok = true;
#pragma unroll
        for (int k = 0; k < K; ++k) { const int i = lane + 64 * k; ok &= sy_ld(p + (i < n ? i : n - 1)) == tag; }
        if (__all(ok)) break;
        __builtin_amdgcn_s_sleep(1);
    }
    asm volatile("" ::: "memory");
}
DI void publish(GAS unsigned* p, unsigned tag, int lane) { WAIT_VM(0); if (lane == 0) sy_st(p, tag); }
DI float dot2bf(unsigned a, unsigned b, float c) { return __builtin_amdgcn_fdot2_f32_bf16(__builtin_bit_cast(bf16x2, a), __builtin_bit_cast(bf16x2, b), c, false); }

template <int CTRL, int RM> DI float dpp_add(float v) { return v + __builtin_bit_cast(float, __builtin_amdgcn_update_dpp(0, __builtin_bit_cast(int, v), CTRL, RM, 0xf, true)); }
DI float wave_sum(float v) {
    v = dpp_add<0x111, 0xf>(v); v = dpp_add<0x112, 0xf>(v); v = dpp_add<0x114, 0xf>(v); v = dpp_add<0x118, 0xf>(v);
    v = dpp_add<0x142, 0xa>(v); v = dpp_add<0x143, 0xc>(v);
    return __builtin_bit_cast(float, __builtin_amdgcn_readlane(__builtin_bit_cast(int, v), 63));
}
DI float sum_8_16_32(float v) {
    v = dpp_add<0x128, 0xf>(v);
    auto a = __builtin_amdgcn_permlane16_swap(__float_as_uint(v), __float_as_uint(v), false, false); v = __uint_as_float(a[0]) + __uint_as_float(a[1]);
    auto b = __builtin_amdgcn_permlane32_swap(__float_as_uint(v), __float_as_uint(v), false, false); return __uint_as_float(b[0]) + __uint_as_float(b[1]);
}

struct Params { cu16p x, lar, aim, cre, cim, ldt, dsk, wg, wu, wd; unsigned short* out; char* ws; };

struct RowIn { unsigned v[5]; };
DI void row_load(RowIn& a, cu16p x, int row, int lane) {
#pragma unroll
    for (int i = 0; i < 5; ++i) a.v[i] = ((const unsigned*)(x + (size_t)row * D_))[64 * i + lane];
}
DI void row_stats(const RowIn& a, int row, int lane, unsigned tag, char* ws) {
    float s = 0.f;
#pragma unroll
    for (int i = 0; i < 5; ++i) s += bflo(a.v[i]) + bfhi(a.v[i]);
    const float mean = wave_sum(s) * (1.f / D_);
    float q = 0.f;
#pragma unroll
    for (int i = 0; i < 5; ++i) { const float u = bflo(a.v[i]) - mean, v = bfhi(a.v[i]) - mean; q += u * u + v * v; }
    const float sd = sqrtf(rbf(rbf(wave_sum(q) * (1.f / D_)) + 1e-5f));
    if (lane == 0) __hip_atomic_store((GAS unsigned long long*)(ws + WS_STATS) + row, ((unsigned long long)tag << 32) | pk_bf16(mean, sd), __ATOMIC_RELAXED, __HIP_MEMORY_SCOPE_AGENT);
}
template <int NG> DI void stats_poll(unsigned (&g)[NG], srd_t sws, unsigned vo, unsigned so, unsigned tag) {
#pragma unroll 1
    for (int it = 0; it < (1 << 18); ++it) {
        bool ok = true;
#pragma unroll
        for (int k = 0; k < NG; ++k) { const u32x2 t = ld8_dev(sws, vo + 8 * k, so); g[k] = t[0]; ok &= t[1] == tag; }
        if (__all(ok)) break;
        __builtin_amdgcn_s_sleep(1);
    }
}
DI void lnt_issue(int u, int lane, unsigned ldsaddr, srd_t sx) {
    const int blk = u & 31, cb = u >> 5, r4 = lane >> 4;
#pragma unroll
    for (int i = 0; i < 16; ++i) dma16(ldsaddr + i * 1024, (unsigned)(r4 * 1280 + 16 * ((lane & 15) ^ ((((i >> 1) & 3) << 2) | r4))), sx, (unsigned)((blk * 64 + 4 * i) * 1280 + cb * 256));
}
DI void lnt_finish(int u, const unsigned (&g)[8], int lane, lds_ptr lds, srd_t sws) {
    const int blk = u & 31, cb = u >> 5, gq = lane >> 4, q = (lane & 15) >> 2, pp = lane & 3, oc = 2 * gq + (pp >> 1);
    float rs[8], nm[8];
#pragma unroll
    for (int e = 0; e < 8; ++e) { rs[e] = __builtin_amdgcn_rcpf(bfhi(g[e])); nm[e] = -bflo(g[e]) * rs[e]; }
    const unsigned ra = (8 * oc + q) * 256 + 8 * (pp & 1), fx = 16 * (((oc & 3) << 2) | q);
    const unsigned vo = (lane & 7) * 128 + (2 * gq + ((lane >> 3) & 1)) * 16;
    u32x2 tl[16], th[16];
#pragma unroll
    for (int k = 0; k < 16; ++k) { tl[k] = __builtin_bit_cast(u32x2, tr_read(lds + ra + ((16 * k) ^ fx))); th[k] = __builtin_bit_cast(u32x2, tr_read(lds + ra + 1024 + ((16 * k) ^ fx))); }
#pragma unroll
    for (int k = 0; k < 16; ++k) {
        const u32x2 lo = tl[k], hi = th[k];
        const u32x4 o = {pk_f16(fmaf(bflo(lo[0]), rs[0], nm[0]), fmaf(bfhi(lo[0]), rs[1], nm[1])), pk_f16(fmaf(bflo(lo[1]), rs[2], nm[2]), fmaf(bfhi(lo[1]), rs[3], nm[3])),
                         pk_f16(fmaf(bflo(hi[0]), rs[4], nm[4]), fmaf(bfhi(hi[0]), rs[5], nm[5])), pk_f16(fmaf(bflo(hi[1]), rs[6], nm[6]), fmaf(bfhi(hi[1]), rs[7], nm[7]))};
        st16_dev(o, sws, vo, WS_UT + (unsigned)(blk * D_ + cb * 128) * 128 + k * 1024);
    }
}

struct CM { f32x2 d, n; };
DI CM cm(f32x2 w) { return CM{f32x2{w[0], w[0]}, f32x2{-w[1], w[1]}}; }
DI f32x2 cmul(f32x2 x, const CM& m) { return __builtin_elementwise_fma(__builtin_shufflevector(x, x, 1, 0), m.n, x * m.d); }
DI f32x2 cexpp(float er, float rev, float pos) {
    const float m = __builtin_amdgcn_exp2f(er * pos);
    const float t = __builtin_amdgcn_fractf(rev * pos);
    return f32x2{m * __builtin_amdgcn_cosf(t), m * __builtin_amdgcn_sinf(t)};
}
constexpr int KG_ROW = 272, KG_BYTES = 32 * KG_ROW + 784;
struct KgIn { unsigned short lar[2], aim[2], cre[2], cim[2], ldt, dsk; };
DI void kgen_load(KgIn& k, const Params& p, int d, int lane) {
#pragma unroll
    for (int hf = 0; hf < 2; ++hf) { const int n = d * N_ + 64 * hf + lane; k.lar[hf] = p.lar[n]; k.aim[hf] = p.aim[n]; k.cre[hf] = p.cre[n]; k.cim[hf] = p.cim[n]; }
    k.ldt = p.ldt[d]; k.dsk = p.dsk[d];
}
DI void kgen_run(const KgIn& k, int d, int lane, lds_ptr lds, srd_t sws) {
    const float dt = rbf(expf(bf2f(k.ldt))), dk = bf2f(k.dsk);
    float er[2], rv[2], cr[2], ci[2];
#pragma unroll
    for (int hf = 0; hf < 2; ++hf) {
        er[hf] = (-rbf(expf(bf2f(k.lar[hf]))) * dt) * 1.44269504088896341f;
        rv[hf] = (bf2f(k.aim[hf]) * dt) * 0.15915494309189535f;
        cr[hf] = bf2f(k.cre[hf]) * dt; ci[hf] = bf2f(k.cim[hf]) * dt;
    }
    const float bound = 2.f * wave_sum(fabsf(cr[0]) + fabsf(ci[0]) + fabsf(cr[1]) + fabsf(ci[1])) + fabsf(dk);
    int e = (int)(__float_as_uint(bound) >> 23) - 127;
    e = e < -100 ? -100 : (e > 100 ? 100 : e);
    const float S = __uint_as_float((unsigned)(127 + 13 - e) << 23), invS = __uint_as_float((unsigned)(127 - 13 + e) << 23);
    f32x16 acc = {};
    lds_ptr TW = lds + lane * 4, TR = lds + (lane & 31) * KG_ROW + (lane >> 5) * 16, TS = lds + 32 * KG_ROW;
    float er_ = er[0], rv_ = rv[0], cr_ = cr[0], ci_ = ci[0];
#pragma unroll 1
    for (int hf = 0; hf < 2; ++hf) {
#define PUTA(row, g) *(LAS unsigned*)(TW + (row) * KG_ROW) = pk_f16((g)[0], -(g)[1])
#define PUTB(row, g) *(LAS unsigned*)(TW + (row) * KG_ROW) = pk_f16((g)[0], (g)[1])
        const f32x2 w8 = cexpp(er_, rv_, 8.f), w32 = cexpp(er_, rv_, 32.f);
        const CM m8 = cm(w8), m32 = cm(w32);
        f32x2 eb[4]; eb[0] = w32; eb[1] = w8; eb[2] = cmul(w8, m8); eb[3] = cmul(eb[2], m8);
        PUTB(0, (f32x2{1.f, 0.f})); PUTB(1, eb[1]); PUTB(2, eb[2]); PUTB(3, eb[3]); PUTB(4, eb[0]);
#pragma unroll
        for (int a = 1; a < 8; ++a)
#pragma unroll
            for (int b = 0; b < 4; ++b) if (a > 1 || b > 0) { eb[b] = cmul(eb[b], m32); PUTB(4 * a + b, eb[b]); }
        const f32x2 w1 = cexpp(er_, rv_, 1.f), w256 = cexpp(er_, rv_, 256.f);
        const CM m1 = cm(w1), m256 = cm(w256);
        const f32x2 w2 = cmul(w1, m1); const CM m2 = cm(w2), m4 = cm(cmul(w2, m2)), m512 = cm(cmul(w256, m256));
        f32x2 g[21];
        g[0] = f32x2{2.f * S * cr_, 2.f * S * ci_};
        g[1] = cmul(g[0], m1); g[2] = cmul(g[0], m2); g[8] = cmul(g[0], m256); g[12] = cmul(g[0], m512); g[3] = cmul(g[1], m2);
        g[4] = cmul(g[0], m4); g[5] = cmul(g[1], m4); g[6] = cmul(g[2], m4); g[7] = cmul(g[3], m4);
        g[9] = cmul(g[8], m2); g[10] = cmul(g[8], m4); g[14] = cmul(g[8], m512); g[16] = cmul(g[12], m512); g[13] = cmul(g[12], m4);
        g[11] = cmul(g[9], m4); g[15] = cmul(g[14], m4); g[17] = cmul(g[14], m512); g[18] = cmul(g[16], m512);
        g[19] = cmul(g[17], m512); g[20] = cmul(g[18], m512);
        WAIT_LG();
        f16x8 bf[8];
#pragma unroll
        for (int ks = 0; ks < 8; ++ks) bf[ks] = *(LAS f16x8*)(TR + ks * 32);
        WAIT_LG();
#pragma unroll
        for (int r = 0; r < 21; ++r) PUTA(r, g[r]);
#undef PUTA
#undef PUTB
        WAIT_LG();
#pragma unroll
        for (int ks = 0; ks < 8; ++ks) acc = __builtin_amdgcn_mfma_f32_32x32x16_f16(*(LAS f16x8*)(TR + ks * 32), bf[ks], acc, 0, 0, 0);
        WAIT_LG();
        er_ = er[1]; rv_ = rv[1]; cr_ = cr[1]; ci_ = ci[1];
    }
    const int col = lane & 31, hh = lane >> 5;
    if (lane == 0) { acc[0] += dk * S; __builtin_amdgcn_raw_buffer_store_b32(__float_as_uint(invS), sws, d * 4, WS_KINV, SC1); }
    const unsigned R = WS_KR + (unsigned)d * (KR_STRIDE * 2);
    st8_dev(u32x2{pk_f16(acc[3], acc[2]), pk_f16(acc[1], acc[0])}, sws, 2 * (2044 - 8 * col - 4 * hh), R);
    if (hh == 0) {
        *(LAS u32x2*)(TS + 8 * col) = u32x2{pk_f16(acc[4], acc[5]), pk_f16(acc[6], acc[7])};
#pragma unroll
        for (int i = 0; i < 4; ++i) *(LAS _Float16*)(TS + 2 * (256 + 32 * i + col)) = (_Float16)acc[8 + i];
    } else {
        *(LAS unsigned*)(TS + 2 * (128 + 2 * col)) = pk_f16(acc[4], acc[5]);
        *(LAS unsigned*)(TS + 2 * (192 + 2 * col)) = pk_f16(acc[6], acc[7]);
        if (col == 0) *(LAS unsigned*)(TS + 2 * 384) = pk_f16(acc[8], 0.f);
    }
    WAIT_LG();
    unsigned a0[3], a1[3];
#pragma unroll
    for (int sg = 0; sg < 3; ++sg) { a0[sg] = *(LAS unsigned*)(TS + 256 * sg + 4 * lane); a1[sg] = *(LAS unsigned*)(TS + 256 * sg + 4 * lane + 4); }
#define PK(sg, lo, hi) __builtin_amdgcn_perm(a1[sg], a0[sg], (unsigned)((2 * (lo)) | ((2 * (lo) + 1) << 8) | ((2 * (hi)) << 16) | ((2 * (hi) + 1) << 24)))
    st8_dev(u32x2{PK(0, 2, 1), PK(0, 0, 0)}, sws, 2 * (1788 - 4 * lane), R);
    st16_dev(u32x4{PK(1, 2, 2), PK(1, 1, 1), PK(1, 1, 0), PK(1, 0, 0)}, sws, 2 * (1528 - 8 * lane), R);
    { const unsigned t22 = PK(2, 2, 2), t11 = PK(2, 1, 1), t10 = PK(2, 1, 0), t00 = PK(2, 0, 0);
      st16_dev(u32x4{t22, t22, t11, t11}, sws, 2 * (1008 - 16 * lane), R);
      st16_dev(u32x4{t11, t10, t00, t00}, sws, 2 * (1008 - 16 * lane + 8), R); }
#undef PK
    if (lane < 4) st16_dev(u32x4{0, 0, 0, 0}, sws, 2 * (2048 + 8 * lane), R);
    WAIT_LG();
}

constexpr int CV_COPY = 4160, CV_U = 4 * CV_COPY, CV_BYTES = CV_U + 4608;
struct ConvIn { u32x4 e4[4], uu[4]; u32x2 e2[4]; unsigned inv; };
DI void conv_load(ConvIn& c, int d, int lane, srd_t sws) {
    const unsigned R = WS_KR + (unsigned)d * (KR_STRIDE * 2), U = WS_UT + (unsigned)d * 128, vu = (lane >> 3) * (D_ * 128) + (lane & 7) * 16;
#pragma unroll
    for (int i = 0; i < 4; ++i) { c.e4[i] = ld16_dev(sws, 1024 * i + 16 * lane, R); c.e2[i] = ld8_dev(sws, 1024 * i + 16 * lane + 16, R); c.uu[i] = ld16_dev(sws, vu, U + i * (8 * D_ * 128)); }
    c.inv = __builtin_amdgcn_raw_buffer_load_b32(sws, d * 4, WS_KINV, SC1);
}
DI void conv_stage(const ConvIn& c, int lane, lds_ptr lds) {
#pragma unroll
    for (int i = 0; i < 4; ++i) {
        const u32x4 e = c.e4[i]; const u32x2 f = c.e2[i];
        const unsigned o0 = __builtin_amdgcn_alignbit(e[1], e[0], 16), o1 = __builtin_amdgcn_alignbit(e[2], e[1], 16), o2 = __builtin_amdgcn_alignbit(e[3], e[2], 16),
                       o3 = __builtin_amdgcn_alignbit(f[0], e[3], 16), o4 = __builtin_amdgcn_alignbit(f[1], f[0], 16);
        lds_ptr q = lds + 1024 * i + 16 * lane;
        *(LAS u32x4*)(q) = e;
        *(LAS u32x4*)(q + CV_COPY) = u32x4{o0, o1, o2, o3};
        *(LAS u32x4*)(q + 2 * CV_COPY) = u32x4{e[1], e[2], e[3], f[0]};
        *(LAS u32x4*)(q + 3 * CV_COPY) = u32x4{o1, o2, o3, o4};
        *(LAS u32x4*)(q + CV_U + 480) = c.uu[i];
    }
    if (lane < 30) *(LAS u32x4*)(lds + CV_U + 16 * lane) = u32x4{0, 0, 0, 0};
    else if (lane < 46) *(LAS u32x4*)(lds + ((lane - 30) >> 2) * CV_COPY + 4096 + 16 * ((lane - 30) & 3)) = u32x4{0, 0, 0, 0};
    WAIT_LG();
}
DI constexpr int cv_dl(int m) { return m < 8 ? 0 : m < 15 ? 1 : m < 21 ? 2 : m < 26 ? 3 : m < 30 ? 4 : m < 33 ? 5 : m < 35 ? 6 : 7; }
DI constexpr int cv_cb(int m) { const int st[8] = {0, 8, 15, 21, 26, 30, 33, 35}; return 7 - cv_dl(m) - (m - st[cv_dl(m)]); }
DI constexpr int cv_ra(int m) { return m < 2 ? 1 : m == 5 || m == 6 ? 2 : m == 11 || m == 12 ? 3 : m == 17 || m == 18 ? 4 : m == 19 || m == 20 ? 5 : m == 23 || m == 24 ? 6 : m == 25 || m == 26 ? 7 : m == 28 || m == 29 ? 8 : -1; }
DI constexpr int cv_rh(int m) { return m == 1 || m == 6 || m == 12 || m == 18 || m == 20 || m == 24 || m == 26 || m == 29; }
DI constexpr int cv_rb(int m) { return m == 2 ? 7 : m == 9 ? 6 : m == 16 ? 5 : m == 22 ? 4 : m == 27 ? 3 : m == 31 ? 2 : m == 34 ? 1 : m == 35 ? 0 : -1; }
struct ConvRegs { u32x2 al[8], ah[8]; f16x8 b[8]; f32x4 acc[8]; };
template <int S, int M> DI void cv_slot(ConvRegs& r, lds_ptr pa, lds_ptr pb) {
    constexpr int dl = cv_dl(M), cb = cv_cb(M), ra = cv_ra(M), rb = cv_rb(M);
    r.acc[dl + cb] = __builtin_amdgcn_mfma_f32_16x16x32_f16(__builtin_bit_cast(f16x8, u32x4{r.al[dl][0], r.al[dl][1], r.ah[dl][0], r.ah[dl][1]}), r.b[cb], r.acc[dl + cb], 0, 0, 0);
    if constexpr (ra >= 1 && ra <= 7) { if constexpr (cv_rh(M)) r.ah[ra] = *(LAS u32x2*)(pa + 2 * (256 * (7 - ra) + 32 * (7 - S)) + 8); else r.al[ra] = *(LAS u32x2*)(pa + 2 * (256 * (7 - ra) + 32 * (7 - S))); }
    if constexpr (ra == 8 && S < 7) { if constexpr (cv_rh(M)) r.ah[0] = *(LAS u32x2*)(pa + 2 * (256 * 7 + 32 * (6 - S)) + 8); else r.al[0] = *(LAS u32x2*)(pa + 2 * (256 * 7 + 32 * (6 - S))); }
    if constexpr (rb >= 0 && S < 7) r.b[rb] = *(LAS f16x8*)(pb + 2 * (256 * rb + 32 * (6 - S)));
    SB();
}
template <int S, int... M> DI void cv_step(ConvRegs& r, lds_ptr pa, lds_ptr pb, std::integer_sequence<int, M...>) { (cv_slot<S, M>(r, pa, pb), ...); }
template <int... S> DI void cv_steps(ConvRegs& r, lds_ptr pa, lds_ptr pb, std::integer_sequence<int, S...>) { (cv_step<S>(r, pa, pb, std::make_integer_sequence<int, 36>{}), ...); }
DI void conv_run(int d, float inv, int lane, lds_ptr lds, srd_t sws) {
    const int b = lane & 15, h = lane >> 4;
    lds_ptr pa = lds + (3 - (b & 3)) * CV_COPY + 2 * (2052 - 8 * h - 4 * (b >> 2)) - 2 * (256 * 7 + 32 * 7);
    lds_ptr pb = lds + CV_U + 2 * (240 + 16 * b - 8 * h + 8) - 2 * 32 * 7;
    ConvRegs r;
#pragma unroll
    for (int c = 0; c < 8; ++c) r.acc[c] = f32x4{0.f, 0.f, 0.f, 0.f};
    r.al[0] = *(LAS u32x2*)(pa + 2 * (256 * 7 + 32 * 7)); SB(); r.ah[0] = *(LAS u32x2*)(pa + 2 * (256 * 7 + 32 * 7) + 8); SB();
#pragma unroll
    for (int c = 7; c >= 0; --c) r.b[c] = *(LAS f16x8*)(pb + 2 * (256 * c + 32 * 7));
    SB();
    cv_steps(r, pa, pb, std::make_integer_sequence<int, 8>{});
    const unsigned S = WS_ST + (unsigned)d * 128, vs = (b >> 2) * (D_ * 128) + (b & 3) * 32 + 8 * h;
#pragma unroll
    for (int c = 0; c < 8; ++c) st8_dev(u32x2{pk_bf16(r.acc[c][0] * inv, r.acc[c][1] * inv), pk_bf16(r.acc[c][2] * inv, r.acc[c][3] * inv)}, sws, vs, S + c * (4 * D_ * 128));
}

DI void wd_issue(int u, int lane, unsigned ldsaddr, srd_t swd) {
    const int fs = u / 5, cb = u % 5;
#pragma unroll
    for (int i = 0; i < 8; ++i) { const int row = 4 * i + (lane >> 4), p16 = lane & 15;
        dma16(ldsaddr + i * 1024, (unsigned)(row * 1280 + 16 * ((((p16 >> 1) ^ (row & 7)) << 1) | (p16 & 1))), swd, (unsigned)(fs * 32 * 1280 + cb * 256)); }
}
DI void wd_finish(int u, int lane, lds_ptr lds, srd_t sws) {
    const int fs = u / 5, cb = u % 5;
    const int kq = lane >> 4, q = (lane & 15) >> 2, pp = lane & 3, row = 4 * kq + q;
    u32x2 ta[8], tb[8];
#pragma unroll
    for (int i = 0; i < 8; ++i) { ta[i] = __builtin_bit_cast(u32x2, tr_read(lds + row * 256 + 32 * (i ^ (row & 7)) + 8 * pp));
                                  tb[i] = __builtin_bit_cast(u32x2, tr_read(lds + (16 + row) * 256 + 32 * (i ^ (row & 7)) + 8 * pp)); }
#pragma unroll
    for (int i = 0; i < 8; ++i) {
        const u32x2 a = ta[i], b = tb[i];
        const int d16 = 8 * cb + i;
        st16_dev(u32x4{a[0], a[1], b[0], b[1]}, sws, lane * 16, WS_WD + (unsigned)(((d16 / 5) * 54 + fs) * 5 + d16 % 5) * 1024);
    }
    WAIT_LG();
}

constexpr int G1_SLOT = 49152, G1_WG = 32768, G1_WU = 40960;
constexpr int AUX = 159744, AUX_CS = AUX, AUX_ST = AUX + 2048, AUX_DONE = AUX - 64, AUX_GO = AUX - 32;
constexpr int G2_HALF = 15360;
DI constexpr int g2_slot(int st) { return (st % 5) < 3 ? 63744 + (st % 5) * 2 * G2_HALF : ((st % 5) - 3) * 2 * G2_HALF; }
struct Frag { u32x4 x[4]; s16x4 wl[4], wh[4]; };
#define MF16(acc, lo, hi, X) acc = __builtin_amdgcn_mfma_f32_16x16x32_bf16(__builtin_bit_cast(bf16x8, s16x8{lo[0], lo[1], lo[2], lo[3], hi[0], hi[1], hi[2], hi[3]}), __builtin_bit_cast(bf16x8, X), acc, 0, 0, 0)
template <int NV> DI void wait_h(u32x4& a) { asm volatile("s_waitcnt vmcnt(%1)" : "+v"(a) : "n"(NV) : "memory"); }
template <int I> using IC = std::integral_constant<int, I>;
#define HD 6
struct G2Regs { u32x4 hb[HD]; bf16x8 w[2][5]; f32x4 acc[5]; };
template <int T> DI void g2_step(G2Regs& r, lds_ptr pw, unsigned vl16, srd_t sws, unsigned soh) {
    constexpr int st = T / 3, nx = T + 1;
    if constexpr (T % 3 == 2 && T != 5 && T < 26) { LBAR(); SB(); }
    wait_h<(T + HD <= 27 ? HD - 1 : 26 - T)>(r.hb[T % HD]); SB();
#pragma unroll
    for (int j = 0; j < 5; ++j) {
        r.acc[j] = __builtin_amdgcn_mfma_f32_16x16x32_bf16(r.w[T & 1][j], __builtin_bit_cast(bf16x8, r.hb[T % HD]), r.acc[j], 0, 0, 0);
        if constexpr (nx < 27) r.w[nx & 1][j] = *(LAS bf16x8*)(pw + g2_slot(nx / 3) + ((nx % 3) * 5 + j) * 1024);
        SB();
    }
    if constexpr (T + HD < 27) { hload(r.hb[T % HD], vl16, sws, soh + (T + HD) * 4096); SB(); }
    (void)st;
}
template <int B, int... T> DI void g2_steps(G2Regs& r, lds_ptr pw, unsigned vl16, srd_t sws, unsigned soh, std::integer_sequence<int, T...>) { (g2_step<B + T>(r, pw, vl16, sws, soh), ...); }

__global__ __launch_bounds__(768, 3) void fused_kernel(Params p) {
    extern __shared__ __attribute__((aligned(16))) char smem[];
    lds_ptr lds = (lds_ptr)smem;
    const unsigned lds0 = (unsigned)(uintptr_t)lds;
    const int tid = threadIdx.x, lane = tid & 63, w = __builtin_amdgcn_readfirstlane(tid >> 6);
    const int xg = blockIdx.x & 7, bi = blockIdx.x >> 3;
    const bool gemm_wg = bi >= 5;
    GAS unsigned* sy = (GAS unsigned*)(p.ws + WS_SYNC);
    const srd_t sws = mk_srd(p.ws, WS_END);
    STAMP_DECL; STAMP(0);
    const int rt = 4 * xg + (bi & 3), dt = bi >> 2;
    const unsigned vl16 = lane * 16;
    auto w2_stage = [&](int st, int first, int step, int np) {
#pragma unroll
        for (int i = 0; i < np; ++i) { const int pc = first + step * i, kh = pc >= 15, pp = pc - 15 * kh;
            dma16_dev(lds0 + g2_slot(st) + pc * 1024, vl16, sws, WS_WD + (unsigned)((dt * 54 + 27 * kh + 3 * st) * 5 + pp) * 1024); }
    };
    unsigned e_raw;
    asm volatile("global_load_dword %0, %1, %2 sc1" : "=v"(e_raw) : "v"(0u), "s"(p.ws + WS_SYNC + 4 * SY_E) : "memory");
#define TAKE_TAG(N) do { asm volatile("s_waitcnt vmcnt(" #N ")\n\tv_readfirstlane_b32 %0, %1\n\ts_nop 4" : "=s"(tag) : "v"(e_raw) : "memory"); tag += 1u; } while (0)
    unsigned tag;
    if (gemm_wg) {
        const int t = (xg & 3) * 27 + (bi - 5), panel = (xg >> 2) * 4 + (t & 3), ft = t >> 2;
        if (w >= 8) {
            const int lw = w - 8;
            const srd_t sx = mk_srd(p.x, L_ * D_ * 2), sg = mk_srd(p.wg, D_ * F_ * 2), su = mk_srd(p.wu, D_ * F_ * 2);
            const unsigned vx0 = (lane >> 3) * 1280 + 16 * ((lane & 7) ^ (lane >> 4)), vx1 = vx0 ^ 64;
            const unsigned vw0 = (lane >> 3) * 3456 + 16 * ((lane & 7) ^ (2 * ((lane >> 4) & 1))), vw1 = vw0 ^ 64;
            const unsigned sox = (unsigned)(panel * 256 + 64 * lw) * 1280, sow = (unsigned)(16 * lw) * 3456 + ft * 128;
            auto tile = [&](int kt, unsigned slot) {
                dma16(lds0 + slot + G1_WG + lw * 2048, vw0, sg, sow + kt * 64 * 3456);
                dma16(lds0 + slot + G1_WU + lw * 2048, vw0, su, sow + kt * 64 * 3456);
                dma16(lds0 + slot + G1_WG + lw * 2048 + 1024, vw1, sg, sow + kt * 64 * 3456 + 8 * 3456);
                dma16(lds0 + slot + G1_WU + lw * 2048 + 1024, vw1, su, sow + kt * 64 * 3456 + 8 * 3456);
#pragma unroll
                for (int i = 0; i < 8; ++i) dma16(lds0 + slot + (8 * lw + i) * 1024, (i & 1) ? vx1 : vx0, sx, sox + i * 8 * 1280 + kt * 128);
            };
            asm volatile("s_nop 4" ::: "memory");
            tile(0, 0); tile(1, G1_SLOT);
            TAKE_TAG(12);
            LBAR();
            if (lw == 0 && lane == 0) sy_st(sy + SY_ACK + blockIdx.x, tag);
            tile(2, 2 * G1_SLOT);
            unsigned sl = 0;
#pragma unroll 1
            for (int kt = 0; kt < 6; ++kt) { WAIT_BAR(12); tile(kt + 3, sl); sl = (sl == 2 * G1_SLOT) ? 0 : sl + G1_SLOT; }
            WAIT_BAR(12);
            const int cl = 3 * (bi - 5) + lw, ch = 80 * xg + cl;
            const bool cv = lw < 3 && cl < 80;
            u32x4 pk0, pk1; unsigned pf;
            { const unsigned vo = lw == 3 ? WS_SYNC + SY_WD * 4 + (lane < 40 ? lane : 39) * 16 : WS_STATS + panel * 2048 + lane * 32;
              hload(pk0, vo, sws, 0u); hload(pk1, vo + 16, sws, 0u); }
            asm volatile("buffer_load_dword %0, %1, %2, %3 offen sc1" : "=v"(pf) : "v"(4u * (lane < 32 ? SY_LN + 32 * ((cv ? ch : 0) >> 7) + lane : SY_K + (cv ? ch : 0))), "s"(sws), "s"(WS_SYNC) : "memory");
            tile(9, 0);
            asm volatile("s_waitcnt vmcnt(12) lgkmcnt(0)\n\ts_barrier" : "+v"(pk0), "+v"(pk1), "+v"(pf) :: "memory");
            const bool ready = cv && !NOCONV && __all(pf == tag);
            ConvIn c;
            if (lw == 3) {
                if (!__all(pk0[0] == tag && pk0[1] == tag && pk0[2] == tag && pk0[3] == tag)) poll_all<3>(sy + SY_WD, 160, tag, lane);
                w2_stage(0, 0, 1, 30);
                WAIT_BAR(30);
            } else {
                if (lw == 2) { unsigned g[4] = {pk0[0], pk0[2], pk1[0], pk1[2]};
                    if (!__all(pk0[1] == tag && pk0[3] == tag && pk1[1] == tag && pk1[3] == tag)) stats_poll<4>(g, sws, lane * 32, WS_STATS + panel * 2048, tag);
#pragma unroll
                    for (int k = 0; k < 4; ++k) *(LAS f32x2*)(lds + AUX_ST + lane * 32 + 8 * k) = f32x2{bflo(g[k]), __builtin_amdgcn_rcpf(bfhi(g[k]))}; }
                if (ready) { conv_load(c, ch, lane, sws); WAIT_BAR(13); } else WAIT_BAR(0);
            }
            LBAR();
            STAMP(1);
            if (NOCONV) { if (cv) publish(sy + SY_S + ch, tag, lane); }
            else if (cv) {
                if (!ready) { poll_all<1>(sy + SY_K + ch, 1, tag, lane); poll_all<1>(sy + SY_LN + 32 * (ch >> 7), 32, tag, lane); conv_load(c, ch, lane, sws); }
                STAMP(2);
                conv_stage(c, lane, lds + lw * CV_BYTES);
                STAMP(3);
                conv_run(ch, __uint_as_float(c.inv), lane, lds + lw * CV_BYTES, sws);
                STAMP(4);
                publish(sy + SY_S + ch, tag, lane);
                STAMP(5);
            }
            if (lw == 3) {
#pragma unroll 1
                for (int it = 0; it < (1 << 22); ++it) {
                    if (__all(__hip_atomic_load((LAS unsigned*)(lds + AUX_DONE) + (lane & 7), __ATOMIC_RELAXED, __HIP_MEMORY_SCOPE_WORKGROUP) != 0u)) break;
                    __builtin_amdgcn_s_sleep(2);
                }
                asm volatile("" ::: "memory");
                w2_stage(1, 0, 1, 30); w2_stage(2, 0, 1, 30); STAMP(2); }
        } else {
            if (tid < 16) *(LAS unsigned*)(lds + AUX_DONE + 4 * tid) = 0u;
            { RowIn a; row_load(a, p.x, 8 * blockIdx.x + w, lane); TAKE_TAG(5); row_stats(a, 8 * blockIdx.x + w, lane, tag, p.ws); }
            const int tg = w >> 1, fg = w & 1, kq = lane >> 4, l16 = lane & 15;
            unsigned ax[2], aw[2];
            { const int row = 64 * tg + l16, q = l16 >> 2, pp = lane & 3;
#pragma unroll
              for (int i = 0; i < 2; ++i) { const int ks = i ^ (tg >> 1);
                  ax[i] = row * 128 + 16 * ((4 * ks + kq) ^ ((row >> 1) & 7));
                  aw[i] = G1_WG + (32 * ks + 8 * kq + q) * 128 + 32 * ((2 * fg + (tg & 1)) ^ ((q >> 1) | ((kq & 1) << 1))) + 8 * pp; } }
            float csg = 0.f, csu = 0.f;
            f32x4 ag[4][2] = {}, au[4][2] = {};
            Frag A, B;
#define RX(N, sl, ks, i) N.x[i] = *(LAS u32x4*)(lds + (sl) + ax[ks] + (i) * 2048)
#define RWL(N, sl, ks, j) N.wl[j] = tr_read(lds + (sl) + (aw[ks] ^ (((j) & 1) * 32)) + ((j) >> 1) * 8192)
#define RWH(N, sl, ks, j) N.wh[j] = tr_read(lds + (sl) + (aw[ks] ^ (((j) & 1) * 32)) + ((j) >> 1) * 8192 + 512)
#define MG(C, i, j) MF16(ag[i][j], C.wl[j], C.wh[j], C.x[i])
#define MU(C, i, j) MF16(au[i][j], C.wl[2 + j], C.wh[2 + j], C.x[i])
#define G_(M, RD, R) do { M; if (RD) { R; } SB(); } while (0)
#define CSUM(acc, v, k) do { const unsigned d_ = __builtin_bit_cast(u32x2, v)[k]; acc += bflo(d_); acc2 += bfhi(d_); asm volatile("" : "+v"(acc), "+v"(acc2)); } while (0)
#define STEPC(C, N, RD, sl, ks) do { \
        { float acc2 = 0.f; \
        G_(MG(C, 0, 0), RD, RX(N, sl, ks, 0));  G_(MU(C, 0, 0); CSUM(csg, C.wl[0], 0), RD, RWL(N, sl, ks, 0)); G_(MG(C, 0, 1), RD, RWH(N, sl, ks, 0)); G_(MU(C, 0, 1); CSUM(csg, C.wl[0], 1), RD, RWL(N, sl, ks, 2)); \
        G_(MG(C, 1, 0), RD, RWH(N, sl, ks, 2)); G_(MU(C, 1, 0); CSUM(csg, C.wh[0], 0), RD, RWL(N, sl, ks, 1)); G_(MG(C, 1, 1), RD, RWH(N, sl, ks, 1)); G_(MU(C, 1, 1); CSUM(csg, C.wh[0], 1), RD, RWL(N, sl, ks, 3)); \
        csg += acc2; } { float acc2 = 0.f; \
        G_(MG(C, 2, 0), RD, RWH(N, sl, ks, 3)); G_(MU(C, 2, 0); CSUM(csu, C.wl[2], 0), RD, RX(N, sl, ks, 1));  G_(MG(C, 2, 1), RD, RX(N, sl, ks, 2));  G_(MU(C, 2, 1); CSUM(csu, C.wl[2], 1), RD, RX(N, sl, ks, 3)); \
        G_(MG(C, 3, 0), 0, (void)0); G_(MU(C, 3, 0); CSUM(csu, C.wh[2], 0), 0, (void)0); G_(MG(C, 3, 1), 0, (void)0); G_(MU(C, 3, 1); CSUM(csu, C.wh[2], 1), 0, (void)0); \
        csu += acc2; } } while (0)
#define STEP(C, N, RD, sl, ks) do { \
        G_(MG(C, 0, 0), RD, RX(N, sl, ks, 0));  G_(MU(C, 0, 0), RD, RWL(N, sl, ks, 0)); G_(MG(C, 0, 1), RD, RWH(N, sl, ks, 0)); G_(MU(C, 0, 1), RD, RWL(N, sl, ks, 2)); \
        G_(MG(C, 1, 0), RD, RWH(N, sl, ks, 2)); G_(MU(C, 1, 0), RD, RWL(N, sl, ks, 1)); G_(MG(C, 1, 1), RD, RWH(N, sl, ks, 1)); G_(MU(C, 1, 1), RD, RWL(N, sl, ks, 3)); \
        G_(MG(C, 2, 0), RD, RWH(N, sl, ks, 3)); G_(MU(C, 2, 0), RD, RX(N, sl, ks, 1));  G_(MG(C, 2, 1), RD, RX(N, sl, ks, 2));  G_(MU(C, 2, 1), RD, RX(N, sl, ks, 3)); \
        G_(MG(C, 3, 0), 0, (void)0); G_(MU(C, 3, 0), 0, (void)0); G_(MG(C, 3, 1), 0, (void)0); G_(MU(C, 3, 1), 0, (void)0); } while (0)
            LBAR();
            STAMP(1);
            RX(A, 0, 0, 0); RWL(A, 0, 0, 0); RWH(A, 0, 0, 0); RWL(A, 0, 0, 2); RWH(A, 0, 0, 2); RWL(A, 0, 0, 1); RWH(A, 0, 0, 1); RWL(A, 0, 0, 3); RWH(A, 0, 0, 3);
            RX(A, 0, 0, 1); RX(A, 0, 0, 2); RX(A, 0, 0, 3);
#pragma unroll
            for (int kt = 0; kt < 9; ++kt) {
                if (NOCS) STEP(A, B, 1, (kt % 3) * G1_SLOT, 1); else STEPC(A, B, 1, (kt % 3) * G1_SLOT, 1);
                LBAR(); SB();
                STEP(B, A, 1, ((kt + 1) % 3) * G1_SLOT, 0);
            }
            STEPC(A, B, 1, 0, 1);
            { auto a = __builtin_amdgcn_permlane16_swap(__float_as_uint(csg), __float_as_uint(csu), false, false);
              const float v = __uint_as_float(a[0]) + __uint_as_float(a[1]);
              auto b = __builtin_amdgcn_permlane32_swap(__float_as_uint(v), __float_as_uint(v), false, false);
              if (lane < 32) *(LAS float*)(lds + AUX_CS + (tg >> 1) * 512 + (lane >> 4) * 256 + (32 * fg + 16 * (tg & 1) + l16) * 4) = __uint_as_float(b[0]) + __uint_as_float(b[1]); }
            SB();
            STEP(B, A, 0, 0, 0);
            STAMP(2);
            LBAR();
            __builtin_amdgcn_s_setprio(1);
            f32x4 cgv[2], cuv[2];
#pragma unroll
            for (int j = 0; j < 2; ++j) { lds_ptr q = lds + AUX_CS + (32 * fg + 16 * (j ^ (tg & 1)) + 4 * kq) * 4;
                cgv[j] = *(LAS f32x4*)q + *(LAS f32x4*)(q + 512); cuv[j] = *(LAS f32x4*)(q + 256) + *(LAS f32x4*)(q + 768); }
            f32x2 stt[4];
#pragma unroll
            for (int i = 0; i < 4; ++i) stt[i] = *(LAS f32x2*)(lds + AUX_ST + (64 * tg + 16 * i + l16) * 8);
            const float K0 = -2.f * 0.7978845608028654f * 1.44269504088896341f, K1 = K0 * 0.044715f;
#pragma unroll
            for (int i = 0; i < 4; ++i) {
                const float r = stt[i][1], ir = __builtin_amdgcn_rcpf(r);
                const f32x2 nm = {-stt[i][0], -stt[i][0]}, c0 = {K0 * r, K0 * r}, c1 = {K1 * r * r * r, K1 * r * r * r}, ir2 = {ir * ir, ir * ir};
                u32x4 o;
#pragma unroll
                for (int e = 0; e < 4; ++e) {
                    const int j = e >> 1, b = 2 * (e & 1);
                    const f32x2 g1 = nm * f32x2{cgv[j][b], cgv[j][b + 1]} + f32x2{ag[i][j][b], ag[i][j][b + 1]};
                    const f32x2 u1 = nm * f32x2{cuv[j][b], cuv[j][b + 1]} + f32x2{au[i][j][b], au[i][j][b + 1]};
                    const f32x2 z = g1 * (g1 * g1 * c1 + c0);
                    const f32x2 ex = {__builtin_amdgcn_exp2f(z[0]), __builtin_amdgcn_exp2f(z[1])};
                    const f32x2 dd = ex * ir2 + ir2;
                    const f32x2 hv = g1 * u1 * f32x2{__builtin_amdgcn_rcpf(dd[0]), __builtin_amdgcn_rcpf(dd[1])};
                    o[e] = pk_bf16(hv[0], hv[1]);
                }
                if (tg & 1) o = u32x4{o[2], o[3], o[0], o[1]};
                st16_dev(o, sws, vl16, WS_H + (unsigned)(((panel * 4 + tg) * 54 + ft * 2 + fg) * 4 + i) * 1024);
            }
            if (lane == 0) __hip_atomic_store((LAS unsigned*)(lds + AUX_DONE) + w, 1u, __ATOMIC_RELAXED, __HIP_MEMORY_SCOPE_WORKGROUP);
            STAMP(3);
            publish(sy + SY_H + 64 * (4 * panel + tg) + 2 * ft + fg, tag, lane);
            STAMP(4);
        }
    } else {
        const int hi = bi * 8 + xg;
        if (w < 8) {
            KgIn k0, k1; RowIn a;
            row_load(a, p.x, 8 * blockIdx.x + w, lane);
            kgen_load(k0, p, 16 * hi + w, lane); kgen_load(k1, p, 16 * hi + 8 + w, lane);
            if (tid == 0) *(LAS unsigned*)(lds + AUX_GO) = 0u;
            TAKE_TAG(0);
            row_stats(a, 8 * blockIdx.x + w, lane, tag, p.ws);
            STAMP(1);
#pragma unroll 1
            for (int i = 0; i < NKG; ++i) {
                kgen_run(k0, 16 * hi + 8 * i + w, lane, lds + w * KG_BYTES, sws);
                if (i == 0) STAMP(2);
                publish(sy + SY_K + 16 * hi + 8 * i + w, tag, lane);
                k0 = k1;
            }
            if (NKG < 2 && lane < 2) sy_st(sy + SY_K + 16 * hi + 8 * lane + w, tag);
            STAMP(4);
        } else {
            const int aw = hi * 4 + (w - 8), off = 8 * KG_BYTES + (w - 8) * 16384;
            const srd_t swd = mk_srd(p.wd, D_ * F_ * 2), sx = mk_srd(p.x, L_ * D_ * 2);
            const bool two = aw + 160 < 270;
            __builtin_amdgcn_s_setprio(1);
            asm volatile("s_nop 4" ::: "memory");
            if (NOHELP) { TAKE_TAG(0); if (lane == 0) { sy_st(sy + SY_WD + aw, tag); sy_st(sy + SY_LN + aw, tag); } } else {
            wd_issue(aw, lane, lds0 + off, swd);
            if (two) wd_issue(aw + 160, lane, lds0 + off + 8192, swd);
            TAKE_TAG(0);
            STAMP(1);
            wd_finish(aw, lane, lds + off, sws);
            if (two) wd_finish(aw + 160, lane, lds + off + 8192, sws);
            lnt_issue(aw, lane, lds0 + off, sx);
            WAIT_VM(16);
            if (lane == 0) sy_st(sy + SY_WD + aw, tag);
            STAMP(2);
            unsigned g[8];
            stats_poll<8>(g, sws, (2 * (lane >> 4) + ((lane >> 3) & 1)) * 64, WS_STATS + (aw & 31) * 512, tag);
            WAIT_VM(0);
            STAMP(3);
            lnt_finish(aw, g, lane, lds + off, sws);
            publish(sy + SY_LN + aw, tag, lane);
            }
            STAMP(4);
            poll_all<3>(sy + SY_WD, 160, tag, lane);
            STAMP(5);
            __builtin_amdgcn_s_setprio(0);
        }
        LBAR();
        if (w == 9 && lane == 0) sy_st(sy + SY_ACK + blockIdx.x, tag);
        if (w == 8 && hi == 0) { poll_all<4>(sy + SY_ACK, 256, tag, lane); if (lane == 0) sy_st(sy + SY_E, tag); }
        if (w == 11) { w2_stage(0, 0, 1, 30); w2_stage(1, 0, 1, 30); w2_stage(2, 0, 1, 30); }
    }
    if (w >= 8) {
        const int lw = w - 8;
        if (lw == 3) {
            WAIT_VM(0);
            STAMP(3);
            poll_all<1>(sy + SY_H + 64 * rt, 54, tag, lane);
            STAMP(4);
            if (lane == 0) __hip_atomic_store((LAS unsigned*)(lds + AUX_GO), 1u, __ATOMIC_RELAXED, __HIP_MEMORY_SCOPE_WORKGROUP);
        }
        LBAR();
        STAMP(6);
        if (lw > 0) {
            w2_stage(3, lw - 1, 3, 10); w2_stage(4, lw - 1, 3, 10); w2_stage(5, lw - 1, 3, 10);
            WAIT_BAR(20); w2_stage(6, lw - 1, 3, 10); w2_stage(7, lw - 1, 3, 10);
            WAIT_BAR(30); w2_stage(8, lw - 1, 3, 10);
            WAIT_BAR(30); WAIT_BAR(20); WAIT_BAR(10); WAIT_BAR(0);
        } else {
            LBAR(); LBAR(); LBAR();
            poll_all<2>(sy + SY_S + 80 * dt, 80, tag, lane);
            STAMP(7);
#pragma unroll
            for (int i = 0; i < 10; ++i) dma16_dev(lds0 + g2_slot(4) + i * 1024, vl16, sws, WS_ST + (unsigned)(rt * D_ + 80 * dt) * 128 + i * 1024);
            LBAR(); LBAR(); LBAR();
            WAIT_VM(0);
        }
        LBAR();
        STAMP(8);
        STAMP_FLUSH(p.ws, (gemm_wg ? STWG : STWH));
    } else {
        const int tt = w & 3, kh = w >> 2, q = lane >> 4;
        const unsigned soh = WS_H + (unsigned)((rt * 54 + 27 * kh) * 4 + tt) * 1024;
        const int token = rt * 64 + tt * 16 + (lane & 15), j0 = kh ? 3 : 0;
        u32x2 xv[3];
#pragma unroll
        for (int j = 0; j < 3; ++j) xv[j] = *(const u32x2*)(p.x + (size_t)token * D_ + 80 * dt + 16 * (j0 + j < 5 ? j0 + j : 4) + 4 * q);
        asm volatile("" : "+v"(xv[0]), "+v"(xv[1]), "+v"(xv[2]));
        __builtin_amdgcn_s_setprio(3);
#pragma unroll 1
        for (int it = 0; it < (1 << 22); ++it) {
            if (__builtin_amdgcn_readfirstlane(__hip_atomic_load((LAS unsigned*)(lds + AUX_GO), __ATOMIC_RELAXED, __HIP_MEMORY_SCOPE_WORKGROUP))) break;
            __builtin_amdgcn_s_sleep(1);
        }
        asm volatile("" ::: "memory");
        STAMP(5);
        G2Regs r;
        lds_ptr pw = lds + kh * G2_HALF + vl16;
#pragma unroll
        for (int k = 0; k < HD; ++k) { hload(r.hb[k], vl16, sws, soh + k * 4096);
            if (k == 1) __builtin_amdgcn_s_setprio(2); if (k == 3) __builtin_amdgcn_s_setprio(1); }
#pragma unroll
        for (int j = 0; j < 5; ++j) { r.acc[j] = f32x4{0.f, 0.f, 0.f, 0.f}; r.w[0][j] = *(LAS bf16x8*)(pw + g2_slot(0) + j * 1024); }
        SB();
        g2_steps<0>(r, pw, vl16, sws, soh, std::make_integer_sequence<int, 2>{});
        G2STAMP(6);
        g2_steps<2>(r, pw, vl16, sws, soh, std::make_integer_sequence<int, 1>{});
        G2STAMP(7);
        g2_steps<3>(r, pw, vl16, sws, soh, std::make_integer_sequence<int, 5>{});
        G2STAMP(11);
        g2_steps<8>(r, pw, vl16, sws, soh, std::make_integer_sequence<int, 1>{});
        G2STAMP(12);
        g2_steps<9>(r, pw, vl16, sws, soh, std::make_integer_sequence<int, 6>{});
        G2STAMP(13);
        g2_steps<15>(r, pw, vl16, sws, soh, std::make_integer_sequence<int, 3>{});
        G2STAMP(14);
        g2_steps<18>(r, pw, vl16, sws, soh, std::make_integer_sequence<int, 9>{});
        STAMP(8);
        lds_ptr px = lds + g2_slot(0) + tt * 5120 + vl16;
        if (kh) { *(LAS f32x4*)(px) = r.acc[0]; *(LAS f32x4*)(px + 1024) = r.acc[1]; *(LAS f32x4*)(px + 2048) = r.acc[2]; }
        else    { *(LAS f32x4*)(px + 3072) = r.acc[3]; *(LAS f32x4*)(px + 4096) = r.acc[4]; }
        LBAR();
        STAMP(9);
        auto fin = [&](int j, f32x4 a, u32x2 xx) {
            a += *(LAS f32x4*)(px + j * 1024);
            const s16x4 sv = tr_read(lds + g2_slot(4) + (16 * j + 4 * q + ((lane & 15) >> 2)) * 128 + tt * 32 + (lane & 3) * 8);
            float o[4];
#pragma unroll
            for (int i = 0; i < 4; ++i) {
                const float sg = a[i] * __builtin_amdgcn_rcpf(1.f + __builtin_amdgcn_exp2f(-1.44269504088896341f * a[i]));
                o[i] = fmaf(bf2f((unsigned short)sv[i]), sg, (i & 1) ? bfhi(xx[i >> 1]) : bflo(xx[i >> 1]));
            }
            *(u32x2*)(p.out + (size_t)token * D_ + 80 * dt + 16 * j + 4 * q) = u32x2{pk_bf16(o[0], o[1]), pk_bf16(o[2], o[3])};
        };
        if (kh == 0) { fin(0, r.acc[0], xv[0]); fin(1, r.acc[1], xv[1]); fin(2, r.acc[2], xv[2]); } else { fin(3, r.acc[3], xv[0]); fin(4, r.acc[4], xv[1]); }
        STAMP(10);
        STAMP_FLUSH(p.ws, (gemm_wg ? STWG : STWH));
    }
}

extern "C" void kernel_launch(void* const* d_in, const int* in_sizes, int n_in, void* d_out, int out_size, void* d_ws, size_t ws_size,
                              hipStream_t stream) {
    Params p;
    p.x = (cu16p)d_in[0]; p.lar = (cu16p)d_in[1]; p.aim = (cu16p)d_in[2]; p.cre = (cu16p)d_in[3]; p.cim = (cu16p)d_in[4];
    p.ldt = (cu16p)d_in[5]; p.dsk = (cu16p)d_in[6]; p.wg = (cu16p)d_in[8]; p.wu = (cu16p)d_in[9]; p.wd = (cu16p)d_in[10];
    p.out = (unsigned short*)d_out; p.ws = (char*)d_ws;
    (void)hipFuncSetAttribute((const void*)fused_kernel, hipFuncAttributeMaxDynamicSharedMemorySize, LDS_BYTES);
    fused_kernel<<<256, 768, LDS_BYTES, stream>>>(p);
}
```

```cpp
#include <hip/hip_runtime.h>
#include <hip/hip_bf16.h>
#include <stdint.h>
#include <type_traits>
#include <utility>

#define DI __device__ __forceinline__
#define LAS __attribute__((address_space(3)))
#define GAS __attribute__((address_space(1)))
typedef short s16x8 __attribute__((ext_vector_type(8)));
typedef short s16x4 __attribute__((ext_vector_type(4)));
typedef __bf16 bf16x8 __attribute__((ext_vector_type(8)));
typedef __bf16 bf16x2 __attribute__((ext_vector_type(2)));
typedef _Float16 f16x8 __attribute__((ext_vector_type(8)));
typedef _Float16 f16x2 __attribute__((ext_vector_type(2)));
typedef float f32x16 __attribute__((ext_vector_type(16)));
typedef float f32x4 __attribute__((ext_vector_type(4)));
typedef float f32x2 __attribute__((ext_vector_type(2)));
typedef unsigned u32x4 __attribute__((ext_vector_type(4)));
typedef unsigned u32x2 __attribute__((ext_vector_type(2)));
typedef LAS char* lds_ptr;
typedef const unsigned short* cu16p;
typedef __amdgpu_buffer_rsrc_t srd_t;

constexpr int L_ = 2048, D_ = 640, N_ = 128, F_ = 1728;
constexpr unsigned WS_SYNC = 0;
constexpr unsigned WS_STATS = 512u << 10;
constexpr unsigned WS_KT = 576u << 10;
constexpr unsigned WS_H = 2u << 20;
constexpr unsigned WS_UT = 9u << 20;
constexpr unsigned WS_ST = 12u << 20;
constexpr unsigned WS_WD = 15u << 20;
constexpr unsigned WS_END = 18u << 20;
constexpr size_t WS_STAMP = (size_t)32 << 20;
constexpr int SY_E = 0, SY_ACK = 1, SY_LN = 257, SY_WD = 417, SY_ALL = 577, SY_S = 585, SY_K = 1225, SY_H = 1865;
static_assert((SY_H + 2048) * 128 <= WS_STATS, "sync map");
constexpr int SC1 = 16;
constexpr int LDS_BYTES = 163840;

#define STAMP_DECL do {} while (0)
#define STAMP(i) do {} while (0)
#define STAMP_FLUSH(ws, wv) do {} while (0)
#define TOUCH 1
#define NOCS 0
#define G2STAMP(i) STAMP(i)
#define NOHELP 0
#define HSLEEP 12
#define SL_ST 12
#define SL_K 3
#define SL_S 16
#define SL_H 1
#define SL_HH 16
#define ROT1 1
#define ROT2 0
#define FULLP 22
#define WNT 1
#define WMODE 3
#define HD 4
#define WLAG 4
#define GSLEEP 2
#define PNT 1
#define WSLEEP 0
#define WF1 10
#define T1HOLD 0
#define W3EARLY 1
#define HPRIO 1
#define NKG 2
#define NOCONV 0
#define WLATE 0
#define SB() __builtin_amdgcn_sched_barrier(0)

DI float bf2f(unsigned short v) { return __uint_as_float((unsigned)v << 16); }
DI float bflo(unsigned v) { return __uint_as_float(v << 16); }
DI float bfhi(unsigned v) { return __uint_as_float(v & 0xffff0000u); }
DI float rbf(float x) { return (float)(__bf16)x; }
DI unsigned pk_bf16(float lo, float hi) { bf16x2 v = {(__bf16)lo, (__bf16)hi}; return __builtin_bit_cast(unsigned, v); }
DI unsigned pk_f16(float lo, float hi) { f16x2 v = {(_Float16)lo, (_Float16)hi}; return __builtin_bit_cast(unsigned, v); }
DI s16x4 tr_read(lds_ptr p) { return __builtin_amdgcn_ds_read_tr16_b64_v4i16((LAS s16x4*)p); }
DI srd_t mk_srd(const void* p, unsigned bytes) { return __builtin_amdgcn_make_buffer_rsrc((void*)p, (short)0, (int)bytes, 0x00020000); }
DI void dma16(unsigned ldsaddr, unsigned voff, srd_t srd, unsigned soff) {
    asm volatile("s_mov_b32 m0, %0\n\ts_nop 0\n\tbuffer_load_dwordx4 %1, %2, %3 offen lds" :: "s"(ldsaddr), "v"(voff), "s"(srd), "s"(soff) : "memory");
}
DI void dma16_nt(unsigned ldsaddr, unsigned voff, srd_t srd, unsigned soff) {
    if (WNT) asm volatile("s_mov_b32 m0, %0\n\ts_nop 0\n\tbuffer_load_dwordx4 %1, %2, %3 offen nt lds" :: "s"(ldsaddr), "v"(voff), "s"(srd), "s"(soff) : "memory");
    else dma16(ldsaddr, voff, srd, soff);
}
DI void dma16_dev(unsigned ldsaddr, unsigned voff, srd_t srd, unsigned soff) {
    asm volatile("s_mov_b32 m0, %0\n\ts_nop 0\n\tbuffer_load_dwordx4 %1, %2, %3 offen sc1 lds" :: "s"(ldsaddr), "v"(voff), "s"(srd), "s"(soff) : "memory");
}
DI void hload(u32x4& dst, unsigned vo, srd_t srd, unsigned so) { asm volatile("buffer_load_dwordx4 %0, %1, %2, %3 offen sc1" : "=v"(dst) : "v"(vo), "s"(srd), "s"(so) : "memory"); }
DI u32x4 ld16_dev(srd_t s, unsigned vo, unsigned so) { return __builtin_amdgcn_raw_buffer_load_b128(s, vo, so, SC1); }
DI u32x2 ld8_dev(srd_t s, unsigned vo, unsigned so) { return __builtin_amdgcn_raw_buffer_load_b64(s, vo, so, SC1); }
DI void st16_dev(u32x4 v, srd_t s, unsigned vo, unsigned so) {
    asm volatile("buffer_store_dwordx4 %0, %1, %2, %3 offen sc1\n\ts_nop 1" :: "v"(v), "v"(vo), "s"(s), "s"(so)); }
DI void st8_dev(u32x2 v, srd_t s, unsigned vo, unsigned so) { __builtin_amdgcn_raw_buffer_store_b64(v, s, vo, so, SC1); }
#define WAIT_BAR(N) asm volatile("s_waitcnt vmcnt(" #N ") lgkmcnt(0)\n\ts_barrier" ::: "memory")
#define LBAR() asm volatile("s_waitcnt lgkmcnt(0)\n\ts_barrier" ::: "memory")
#define WAIT_VM(N) asm volatile("s_waitcnt vmcnt(" #N ")" ::: "memory")
#define WAIT_LG() asm volatile("s_waitcnt lgkmcnt(0)" ::: "memory")
struct alignas(128) SyncLine { unsigned v; };
typedef GAS SyncLine* syp;
DI unsigned sy_ld(syp p) { return __hip_atomic_load(&p->v, __ATOMIC_RELAXED, __HIP_MEMORY_SCOPE_AGENT); }
DI void sy_st(syp p, unsigned v) { __hip_atomic_store(&p->v, v, __ATOMIC_RELAXED, __HIP_MEMORY_SCOPE_AGENT); }
template <int K, int SLP = 2> DI void poll_all(syp p, int n, unsigned tag, int lane) {
    for (int it = 0; it < (1 << 18); ++it) {
        bool ok = true;
#pragma unroll
        for (int k = 0; k < K; ++k) { const int i = lane + 64 * k; ok &= sy_ld(p + (i < n ? i : n - 1)) == tag; }
        if (__all(ok)) break;
        __builtin_amdgcn_s_sleep(SLP);
    }
    asm volatile("" ::: "memory");
}
DI unsigned lds_ld(lds_ptr p) { return __hip_atomic_load((LAS unsigned*)p, __ATOMIC_RELAXED, __HIP_MEMORY_SCOPE_WORKGROUP); }
DI void lds_st(lds_ptr p, unsigned v) { __hip_atomic_store((LAS unsigned*)p, v, __ATOMIC_RELAXED, __HIP_MEMORY_SCOPE_WORKGROUP); }
DI void lds_wait(lds_ptr p, int n, unsigned v, int lane) {
#pragma unroll 1
    for (int it = 0; it < (1 << 22); ++it) {
        if (__all(lds_ld(p + 4 * (lane & (n - 1))) >= v)) break;
        __builtin_amdgcn_s_sleep(1);
    }
    asm volatile("" ::: "memory");
}
DI void publish(syp p, unsigned tag, int lane) { WAIT_VM(0); if (lane == 0) sy_st(p, tag); }
DI float dot2bf(unsigned a, unsigned b, float c) { return __builtin_amdgcn_fdot2_f32_bf16(__builtin_bit_cast(bf16x2, a), __builtin_bit_cast(bf16x2, b), c, false); }

template <int CTRL, int RM> DI float dpp_add(float v) { return v + __builtin_bit_cast(float, __builtin_amdgcn_update_dpp(0, __builtin_bit_cast(int, v), CTRL, RM, 0xf, true)); }
DI float wave_sum(float v) {
    v = dpp_add<0x111, 0xf>(v); v = dpp_add<0x112, 0xf>(v); v = dpp_add<0x114, 0xf>(v); v = dpp_add<0x118, 0xf>(v);
    v = dpp_add<0x142, 0xa>(v); v = dpp_add<0x143, 0xc>(v);
    return __builtin_bit_cast(float, __builtin_amdgcn_readlane(__builtin_bit_cast(int, v), 63));
}
DI float sum_8_16_32(float v) {
    v = dpp_add<0x128, 0xf>(v);
    auto a = __builtin_amdgcn_permlane16_swap(__float_as_uint(v), __float_as_uint(v), false, false); v = __uint_as_float(a[0]) + __uint_as_float(a[1]);
    auto b = __builtin_amdgcn_permlane32_swap(__float_as_uint(v), __float_as_uint(v), false, false); return __uint_as_float(b[0]) + __uint_as_float(b[1]);
}

struct Params { cu16p x, lar, aim, cre, cim, ldt, dsk, wg, wu, wd; unsigned short* out; char* ws; };

struct RowIn { unsigned v[5]; };
DI void row_load(RowIn& a, cu16p x, int row, int lane) {
#pragma unroll
    for (int i = 0; i < 5; ++i) a.v[i] = ((const unsigned*)(x + (size_t)row * D_))[64 * i + lane];
}
DI unsigned row_stats(const RowIn& a) {
    float s = 0.f;
#pragma unroll
    for (int i = 0; i < 5; ++i) s += bflo(a.v[i]) + bfhi(a.v[i]);
    const float mean = wave_sum(s) * (1.f / D_);
    float q = 0.f;
#pragma unroll
    for (int i = 0; i < 5; ++i) { const float u = bflo(a.v[i]) - mean, v = bfhi(a.v[i]) - mean; q += u * u + v * v; }
    const float sd = sqrtf(rbf(rbf(wave_sum(q) * (1.f / D_)) + 1e-5f));
    return pk_bf16(mean, sd);
}
DI void row_publish(unsigned v, int row, int lane, unsigned tag, char* ws) {
    if (lane == 0) __hip_atomic_store((GAS unsigned long long*)(ws + WS_STATS) + row, ((unsigned long long)tag << 32) | v, __ATOMIC_RELAXED, __HIP_MEMORY_SCOPE_AGENT);
}
template <int NG> DI void stats_poll(unsigned (&g)[NG], srd_t sws, unsigned vo, unsigned so, unsigned tag) {
#pragma unroll 1
    for (int it = 0; it < (1 << 18); ++it) {
        bool ok = true;
#pragma unroll
        for (int k = 0; k < NG; ++k) { const u32x2 t = ld8_dev(sws, vo + 8 * k, so); g[k] = t[0]; ok &= t[1] == tag; }
        if (__all(ok)) break;
        __builtin_amdgcn_s_sleep(SL_ST);
    }
}
DI void lnt_issue(int u, int lane, unsigned ldsaddr, srd_t sx) {
    ldsaddr = __builtin_amdgcn_readfirstlane(ldsaddr);
    const int blk = u & 31, cb = u >> 5, r4 = lane >> 4;
#pragma unroll
    for (int i = 0; i < 16; ++i) dma16(ldsaddr + i * 1024, (unsigned)(r4 * 1280 + 16 * ((lane & 15) ^ ((((i >> 1) & 3) << 2) | r4))), sx, (unsigned)((blk * 64 + 4 * i) * 1280 + cb * 256));
}
DI void lnt_finish(int u, const unsigned (&g)[8], int lane, lds_ptr lds, srd_t sws) {
    const int blk = u & 31, cb = u >> 5, gq = lane >> 4, q = (lane & 15) >> 2, pp = lane & 3, oc = 2 * gq + (pp >> 1);
    f32x2 rs[4], nm[4];
#pragma unroll
    for (int e = 0; e < 4; ++e) { rs[e] = f32x2{__builtin_amdgcn_rcpf(bfhi(g[2 * e])), __builtin_amdgcn_rcpf(bfhi(g[2 * e + 1]))}; nm[e] = -f32x2{bflo(g[2 * e]), bflo(g[2 * e + 1])} * rs[e]; }
    const unsigned ra = (8 * oc + q) * 256 + 8 * (pp & 1), fx = 16 * (((oc & 3) << 2) | q);
    lds_ptr wo = lds + (lane & 7) * 128 + 16 * ((2 * gq + ((lane >> 3) & 1)) ^ (lane & 7)), ro = lds + (lane >> 3) * 128 + 16 * ((lane & 7) ^ (lane >> 3));
    u32x2 tl[16], th[16];
#pragma unroll
    for (int k = 0; k < 16; ++k) { tl[k] = __builtin_bit_cast(u32x2, tr_read(lds + ra + ((16 * k) ^ fx))); th[k] = __builtin_bit_cast(u32x2, tr_read(lds + ra + 1024 + ((16 * k) ^ fx))); }
    WAIT_LG();
#pragma unroll
    for (int k = 0; k < 16; ++k) {
        const unsigned in[4] = {tl[k][0], tl[k][1], th[k][0], th[k][1]};
        u32x4 o;
#pragma unroll
        for (int e = 0; e < 4; ++e) { f32x2 t = __builtin_elementwise_fma(f32x2{bflo(in[e]), bfhi(in[e])}, rs[e], nm[e]); asm("" : "+v"(t)); o[e] = pk_f16(t[0], t[1]); }
        *(LAS u32x4*)(wo + k * 1024) = o;
    }
    WAIT_LG();
    u32x4 o[16];
#pragma unroll
    for (int k = 0; k < 16; ++k) o[k] = *(LAS u32x4*)(ro + k * 1024);
#pragma unroll
    for (int k = 0; k < 16; ++k) st16_dev(o[k], sws, lane * 16, WS_UT + (unsigned)(blk * D_ + cb * 128) * 128 + k * 1024);
}

struct CM { f32x2 d, n; };
DI CM cm(f32x2 w) { return CM{f32x2{w[0], w[0]}, f32x2{-w[1], w[1]}}; }
DI f32x2 cmul(f32x2 x, const CM& m) { return __builtin_elementwise_fma(__builtin_shufflevector(x, x, 1, 0), m.n, x * m.d); }
DI f32x2 cexpp(float er, float rev, float pos) {
    const float m = __builtin_amdgcn_exp2f(er * pos);
    const float t = __builtin_amdgcn_fractf(rev * pos);
    return f32x2{m * __builtin_amdgcn_cosf(t), m * __builtin_amdgcn_sinf(t)};
}
constexpr int KG_ROW = 272, KG_BYTES = 32 * KG_ROW, KT_BYTES = 1312, KT_S = 512, KT_INV = 1296;
struct KgIn { unsigned lar, aim, cre, cim, ldt, dsk; };
DI void kgen_load(KgIn& k, const Params& p, int d, int lane) {
    const int n = d * (N_ / 2) + lane;
    if (PNT) { k.lar = __builtin_nontemporal_load((const unsigned*)p.lar + n); k.aim = __builtin_nontemporal_load((const unsigned*)p.aim + n);
               k.cre = __builtin_nontemporal_load((const unsigned*)p.cre + n); k.cim = __builtin_nontemporal_load((const unsigned*)p.cim + n); }
    else { k.lar = ((const unsigned*)p.lar)[n]; k.aim = ((const unsigned*)p.aim)[n]; k.cre = ((const unsigned*)p.cre)[n]; k.cim = ((const unsigned*)p.cim)[n]; }
    const unsigned a = ((const unsigned*)p.ldt)[d >> 1], b = ((const unsigned*)p.dsk)[d >> 1];
    k.ldt = (d & 1) ? a & 0xffff0000u : a << 16; k.dsk = (d & 1) ? b & 0xffff0000u : b << 16;
}
struct KgMode { float er, rv, dt; };
DI KgMode kg_mode(const KgIn& k, int hf) {
    const float dt = rbf(expf(__uint_as_float(k.ldt)));
    return KgMode{(-rbf(expf(hf ? bfhi(k.lar) : bflo(k.lar))) * dt) * 1.44269504088896341f, ((hf ? bfhi(k.aim) : bflo(k.aim)) * dt) * 0.15915494309189535f, dt};
}
#define PUTA(row, g) *(LAS unsigned*)(TW + (row) * KG_ROW) = pk_f16((g)[0], -(g)[1])
#define PUTB(row, g) *(LAS unsigned*)(TW + (row) * KG_ROW) = pk_f16((g)[0], (g)[1])
DI void kg_e(const KgMode& m, int lane, lds_ptr tab) {
    lds_ptr TW = tab + lane * 4;
    const f32x2 w8 = cexpp(m.er, m.rv, 8.f), w32 = cexpp(m.er, m.rv, 32.f);
    const CM m8 = cm(w8), m32 = cm(w32);
    f32x2 eb[4]; eb[0] = w32; eb[1] = w8; eb[2] = cmul(w8, m8); eb[3] = cmul(eb[2], m8);
    PUTB(0, (f32x2{1.f, 0.f})); PUTB(1, eb[1]); PUTB(2, eb[2]); PUTB(3, eb[3]); PUTB(4, eb[0]);
#pragma unroll
    for (int a = 1; a < 8; ++a)
#pragma unroll
        for (int b = 0; b < 4; ++b) if (a > 1 || b > 0) { eb[b] = cmul(eb[b], m32); PUTB(4 * a + b, eb[b]); }
}
struct KgG { f32x2 g[21]; float S, invS; };
DI void kg_g(KgG& o, const KgMode& m, const KgIn& k, int hf) {
    const float cr = (hf ? bfhi(k.cre) : bflo(k.cre)) * m.dt, ci = (hf ? bfhi(k.cim) : bflo(k.cim)) * m.dt;
    const float bound = 2.f * m.dt * wave_sum(fabsf(bflo(k.cre)) + fabsf(bfhi(k.cre)) + fabsf(bflo(k.cim)) + fabsf(bfhi(k.cim))) + fabsf(__uint_as_float(k.dsk));
    int e = (int)(__float_as_uint(bound) >> 23) - 127;
    e = e < -100 ? -100 : (e > 100 ? 100 : e);
    o.S = __uint_as_float((unsigned)(127 + 13 - e) << 23); o.invS = __uint_as_float((unsigned)(127 - 13 + e) << 23);
    const f32x2 w1 = cexpp(m.er, m.rv, 1.f), w256 = cexpp(m.er, m.rv, 256.f);
    const CM m1 = cm(w1), m256 = cm(w256);
    const f32x2 w2 = cmul(w1, m1); const CM m2 = cm(w2), m4 = cm(cmul(w2, m2)), m512 = cm(cmul(w256, m256));
    f32x2* g = o.g;
    g[0] = f32x2{2.f * o.S * cr, 2.f * o.S * ci};
    g[1] = cmul(g[0], m1); g[2] = cmul(g[0], m2); g[8] = cmul(g[0], m256); g[12] = cmul(g[0], m512); g[3] = cmul(g[1], m2);
    g[4] = cmul(g[0], m4); g[5] = cmul(g[1], m4); g[6] = cmul(g[2], m4); g[7] = cmul(g[3], m4);
    g[9] = cmul(g[8], m2); g[10] = cmul(g[8], m4); g[14] = cmul(g[8], m512); g[16] = cmul(g[12], m512); g[13] = cmul(g[12], m4);
    g[11] = cmul(g[9], m4); g[15] = cmul(g[14], m4); g[17] = cmul(g[14], m512); g[18] = cmul(g[16], m512);
    g[19] = cmul(g[17], m512); g[20] = cmul(g[18], m512);
}
DI void kg_put_g(const KgG& o, int lane, lds_ptr tab) {
    lds_ptr TW = tab + lane * 4;
#pragma unroll
    for (int r = 0; r < 21; ++r) PUTA(r, o.g[r]);
}
#undef PUTA
#undef PUTB
DI void kg_read(f16x8 (&f)[8], int lane, lds_ptr tab) {
#pragma unroll
    for (int ks = 0; ks < 8; ++ks) f[ks] = *(LAS f16x8*)(tab + (lane & 31) * KG_ROW + (lane >> 5) * 16 + ks * 32);
}
constexpr int KT_STRIDE = 1408;
DI void kgen_channel(const KgIn& k, int d, int lane, lds_ptr tab, lds_ptr kt, srd_t sws, syp prev_flag, unsigned tag) {
    f32x16 acc = {};
    float S, invS;
#pragma unroll 1
    for (int hf = 0; hf < 2; ++hf) {
        const KgMode m = kg_mode(k, hf);
        kg_e(m, lane, tab);
        KgG g; kg_g(g, m, k, hf);
        S = g.S; invS = g.invS;
        WAIT_LG();
        f16x8 a[8], b[8];
        kg_read(b, lane, tab);
        WAIT_LG();
        kg_put_g(g, lane, tab);
        WAIT_LG();
        kg_read(a, lane, tab);
#pragma unroll
        for (int ks = 0; ks < 8; ++ks) acc = __builtin_amdgcn_mfma_f32_32x32x16_f16(a[ks], b[ks], acc, 0, 0, 0);
        WAIT_LG();
        if (prev_flag != nullptr && hf == 0) publish(prev_flag, tag, lane);
    }
    lds_ptr TS = kt + KT_S;
    const int col = lane & 31, hh = lane >> 5;
    if (lane == 0) { acc[0] += __uint_as_float(k.dsk) * S; *(LAS float*)(kt + KT_INV) = invS; }
    *(LAS u32x2*)(kt + 8 * lane) = u32x2{pk_f16(acc[3], acc[2]), pk_f16(acc[1], acc[0])};
    if (hh == 0) {
        *(LAS u32x2*)(TS + 8 * col) = u32x2{pk_f16(acc[4], acc[5]), pk_f16(acc[6], acc[7])};
#pragma unroll
        for (int i = 0; i < 4; ++i) *(LAS _Float16*)(TS + 2 * (256 + 32 * i + col)) = (_Float16)acc[8 + i];
    } else {
        *(LAS unsigned*)(TS + 2 * (128 + 2 * col)) = pk_f16(acc[4], acc[5]);
        *(LAS unsigned*)(TS + 2 * (192 + 2 * col)) = pk_f16(acc[6], acc[7]);
        if (col == 0) *(LAS unsigned*)(TS + 2 * 384) = pk_f16(acc[8], 0.f);
    }
    WAIT_LG();
    const u32x4 o0 = *(LAS u32x4*)(kt + 16 * lane), o1 = *(LAS u32x4*)(kt + 1024 + 16 * (lane < 18 ? lane : 0));
    st16_dev(o0, sws, 16 * lane, WS_KT + d * KT_STRIDE);
    if (lane < 18) st16_dev(o1, sws, 16 * lane, WS_KT + d * KT_STRIDE + 1024);
}

constexpr int CV_COPY = 4160, CV_U = 4 * CV_COPY, CV_BYTES = CV_U + 4608;
struct ConvIn { u32x4 uu[4]; u32x2 z, a[3]; unsigned inv; };
DI void conv_load(ConvIn& c, int d, int lane, srd_t sws) {
    const unsigned U = WS_UT + (unsigned)d * 128, vu = (lane >> 3) * (D_ * 128) + (lane & 7) * 16, T = WS_KT + d * KT_STRIDE;
    c.z = ld8_dev(sws, 8 * lane, T);
#pragma unroll
    for (int sg = 0; sg < 3; ++sg) c.a[sg] = ld8_dev(sws, 4 * lane, T + KT_S + 256 * sg);
    c.inv = __builtin_amdgcn_raw_buffer_load_b32(sws, 0, T + KT_INV, SC1);
#pragma unroll
    for (int i = 0; i < 4; ++i) c.uu[i] = ld16_dev(sws, vu, U + i * (8 * D_ * 128));
}
DI void conv_stage_k(const ConvIn& c, int lane, lds_ptr lds) {
    const unsigned a0[3] = {c.a[0][0], c.a[1][0], c.a[2][0]}, a1[3] = {c.a[0][1], c.a[1][1], c.a[2][1]};
    if (lane < 30) *(LAS u32x4*)(lds + CV_U + 16 * lane) = u32x4{0, 0, 0, 0};
    else if (lane < 46) *(LAS u32x4*)(lds + ((lane - 30) >> 2) * CV_COPY + 4096 + 16 * ((lane - 30) & 3)) = u32x4{0, 0, 0, 0};
    *(LAS u32x2*)(lds + 2 * (2044 - 8 * (lane & 31) - 4 * (lane >> 5))) = c.z;
#define PK(sg, lo, hi) __builtin_amdgcn_perm(a1[sg], a0[sg], (unsigned)((2 * (lo)) | ((2 * (lo) + 1) << 8) | ((2 * (hi)) << 16) | ((2 * (hi) + 1) << 24)))
    *(LAS u32x2*)(lds + 2 * (1788 - 4 * lane)) = u32x2{PK(0, 2, 1), PK(0, 0, 0)};
    *(LAS u32x4*)(lds + 2 * (1528 - 8 * lane)) = u32x4{PK(1, 2, 2), PK(1, 1, 1), PK(1, 1, 0), PK(1, 0, 0)};
    { const unsigned t22 = PK(2, 2, 2), t11 = PK(2, 1, 1), t10 = PK(2, 1, 0), t00 = PK(2, 0, 0);
      *(LAS u32x4*)(lds + 2 * (1008 - 16 * lane)) = u32x4{t22, t22, t11, t11};
      *(LAS u32x4*)(lds + 2 * (1008 - 16 * lane + 8)) = u32x4{t11, t10, t00, t00}; }
#undef PK
    WAIT_LG();
    u32x4 e4[4]; u32x2 e2[4];
#pragma unroll
    for (int i = 0; i < 4; ++i) { e4[i] = *(LAS u32x4*)(lds + 1024 * i + 16 * lane); e2[i] = *(LAS u32x2*)(lds + 1024 * i + 16 * lane + 16); SB(); }
#pragma unroll
    for (int i = 0; i < 4; ++i) {
        const u32x4 e = e4[i]; const u32x2 f = e2[i];
        const unsigned o0 = __builtin_amdgcn_alignbit(e[1], e[0], 16), o1 = __builtin_amdgcn_alignbit(e[2], e[1], 16), o2 = __builtin_amdgcn_alignbit(e[3], e[2], 16),
                       o3 = __builtin_amdgcn_alignbit(f[0], e[3], 16), o4 = __builtin_amdgcn_alignbit(f[1], f[0], 16);
        lds_ptr q = lds + 1024 * i + 16 * lane;
        *(LAS u32x4*)(q + CV_COPY) = u32x4{o0, o1, o2, o3};
        *(LAS u32x4*)(q + 2 * CV_COPY) = u32x4{e[1], e[2], e[3], f[0]};
        *(LAS u32x4*)(q + 3 * CV_COPY) = u32x4{o1, o2, o3, o4};
    }
}
DI void conv_stage_u(const ConvIn& c, int lane, lds_ptr lds) {
#pragma unroll
    for (int i = 0; i < 4; ++i) *(LAS u32x4*)(lds + CV_U + 480 + 1024 * i + 16 * lane) = c.uu[i];
    WAIT_LG();
}
DI constexpr int cv_dl(int m) { return m < 8 ? 0 : m < 15 ? 1 : m < 21 ? 2 : m < 26 ? 3 : m < 30 ? 4 : m < 33 ? 5 : m < 35 ? 6 : 7; }
DI constexpr int cv_cb(int m) { const int st[8] = {0, 8, 15, 21, 26, 30, 33, 35}; return 7 - cv_dl(m) - (m - st[cv_dl(m)]); }
DI constexpr int cv_ra(int m) { return m < 2 ? 1 : m == 5 || m == 6 ? 2 : m == 11 || m == 12 ? 3 : m == 17 || m == 18 ? 4 : m == 19 || m == 20 ? 5 : m == 23 || m == 24 ? 6 : m == 25 || m == 26 ? 7 : m == 28 || m == 29 ? 8 : -1; }
DI constexpr int cv_rh(int m) { return m == 1 || m == 6 || m == 12 || m == 18 || m == 20 || m == 24 || m == 26 || m == 29; }
DI constexpr int cv_rb(int m) { return m == 2 ? 7 : m == 9 ? 6 : m == 16 ? 5 : m == 22 ? 4 : m == 27 ? 3 : m == 31 ? 2 : m == 34 ? 1 : m == 35 ? 0 : -1; }
struct ConvRegs { u32x2 al[8], ah[8]; f16x8 b[8]; f32x4 acc[8]; };
template <int S, int M> DI void cv_slot(ConvRegs& r, lds_ptr pa, lds_ptr pb) {
    constexpr int dl = cv_dl(M), cb = cv_cb(M), ra = cv_ra(M), rb = cv_rb(M);
    r.acc[dl + cb] = __builtin_amdgcn_mfma_f32_16x16x32_f16(__builtin_bit_cast(f16x8, u32x4{r.al[dl][0], r.al[dl][1], r.ah[dl][0], r.ah[dl][1]}), r.b[cb], r.acc[dl + cb], 0, 0, 0);
    if constexpr (ra >= 1 && ra <= 7) { if constexpr (cv_rh(M)) r.ah[ra] = *(LAS u32x2*)(pa + 2 * (256 * (7 - ra) + 32 * (7 - S)) + 8); else r.al[ra] = *(LAS u32x2*)(pa + 2 * (256 * (7 - ra) + 32 * (7 - S))); }
    if constexpr (ra == 8 && S < 7) { if constexpr (cv_rh(M)) r.ah[0] = *(LAS u32x2*)(pa + 2 * (256 * 7 + 32 * (6 - S)) + 8); else r.al[0] = *(LAS u32x2*)(pa + 2 * (256 * 7 + 32 * (6 - S))); }
    if constexpr (rb >= 0 && S < 7) r.b[rb] = *(LAS f16x8*)(pb + 2 * (256 * rb + 32 * (6 - S)));
    SB();
}
template <int S, int... M> DI void cv_step(ConvRegs& r, lds_ptr pa, lds_ptr pb, std::integer_sequence<int, M...>) { (cv_slot<S, M>(r, pa, pb), ...); }
template <int... S> DI void cv_steps(ConvRegs& r, lds_ptr pa, lds_ptr pb, std::integer_sequence<int, S...>) { (cv_step<S>(r, pa, pb, std::make_integer_sequence<int, 36>{}), ...); }
DI void conv_run(int d, float inv, int lane, lds_ptr lds, srd_t sws) {
    const int b = lane & 15, h = lane >> 4;
    lds_ptr pa = lds + (3 - (b & 3)) * CV_COPY + 2 * (2052 - 8 * h - 4 * (b >> 2)) - 2 * (256 * 7 + 32 * 7);
    lds_ptr pb = lds + CV_U + 2 * (240 + 16 * b - 8 * h + 8) - 2 * 32 * 7;
    ConvRegs r;
#pragma unroll
    for (int c = 0; c < 8; ++c) r.acc[c] = f32x4{0.f, 0.f, 0.f, 0.f};
    r.al[0] = *(LAS u32x2*)(pa + 2 * (256 * 7 + 32 * 7)); SB(); r.ah[0] = *(LAS u32x2*)(pa + 2 * (256 * 7 + 32 * 7) + 8); SB();
#pragma unroll
    for (int c = 7; c >= 0; --c) r.b[c] = *(LAS f16x8*)(pb + 2 * (256 * c + 32 * 7));
    SB();
    cv_steps(r, pa, pb, std::make_integer_sequence<int, 8>{});
    const unsigned S = WS_ST + (unsigned)d * 128, vs = (b >> 2) * (D_ * 128) + (b & 3) * 32 + 8 * h;
#pragma unroll
    for (int c = 0; c < 8; ++c) st8_dev(u32x2{pk_bf16(r.acc[c][0] * inv, r.acc[c][1] * inv), pk_bf16(r.acc[c][2] * inv, r.acc[c][3] * inv)}, sws, vs, S + c * (4 * D_ * 128));
}

DI void wd_issue(int u, int lane, unsigned ldsaddr, srd_t swd) {
    ldsaddr = __builtin_amdgcn_readfirstlane(ldsaddr);
    const int fs = u / 5, cb = u % 5;
#pragma unroll
    for (int i = 0; i < 8; ++i) { const int row = 4 * i + (lane >> 4), p16 = lane & 15;
        dma16_nt(ldsaddr + i * 1024, (unsigned)(row * 1280 + 16 * ((((p16 >> 1) ^ (row & 7)) << 1) | (p16 & 1))), swd, (unsigned)(fs * 32 * 1280 + cb * 256)); }
}
DI void wd_finish(int u, int lane, lds_ptr lds, srd_t sws) {
    const int fs = u / 5, cb = u % 5;
    const int kq = lane >> 4, q = (lane & 15) >> 2, pp = lane & 3, row = 4 * kq + q;
    u32x2 ta[8], tb[8];
#pragma unroll
    for (int i = 0; i < 8; ++i) { ta[i] = __builtin_bit_cast(u32x2, tr_read(lds + row * 256 + 32 * (i ^ (row & 7)) + 8 * pp));
                                  tb[i] = __builtin_bit_cast(u32x2, tr_read(lds + (16 + row) * 256 + 32 * (i ^ (row & 7)) + 8 * pp)); }
#pragma unroll
    for (int i = 0; i < 8; ++i) {
        const u32x2 a = ta[i], b = tb[i];
        const int d16 = 8 * cb + i;
        st16_dev(u32x4{a[0], a[1], b[0], b[1]}, sws, lane * 16, WS_WD + (unsigned)(((d16 / 5) * 54 + fs) * 5 + d16 % 5) * 1024);
    }
    WAIT_LG();
}

constexpr int G1_SLOT = 49152, G1_WG = 32768, G1_WU = 40960;
constexpr int AUX_W = LDS_BYTES - 128, AUX_GO = AUX_W, AUX_TAG = AUX_W + 4, AUX_KC = AUX_W + 16, AUX_DONE = AUX_W + 32, AUX_RS = AUX_W + 64;
constexpr int G2_RING = 3 * CV_BYTES, G2_HALF = 15360;
constexpr int AUX_CS = G2_RING + 6 * G2_HALF, AUX_ST = AUX_CS + 1024;
constexpr int g2_slot(int st) { return (st % 5) < 3 ? G2_RING + (st % 5) * 2 * G2_HALF : ((st % 5) - 3) * 2 * G2_HALF; }
constexpr int G2_S = g2_slot(4), G2_X = G2_S + 10240, G2_O = g2_slot(6);
constexpr int WD_TMP = 3 * G1_SLOT;
constexpr int HK_BASE = 65536;
static_assert(AUX_ST + 1024 <= AUX_W && HK_BASE + 8 * (KG_BYTES + KT_BYTES) <= AUX_W, "LDS map");
struct Frag { u32x4 x[4]; s16x4 wl[4], wh[4]; };
#define MF16(acc, lo, hi, X) acc = __builtin_amdgcn_mfma_f32_16x16x32_bf16(__builtin_bit_cast(bf16x8, s16x8{lo[0], lo[1], lo[2], lo[3], hi[0], hi[1], hi[2], hi[3]}), __builtin_bit_cast(bf16x8, X), acc, 0, 0, 0)
template <int NV> DI void wait_h(u32x4& a) { asm volatile("s_waitcnt vmcnt(%1)" : "+v"(a) : "n"(NV) : "memory"); }
template <int I> using IC = std::integral_constant<int, I>;
struct G2Regs { u32x4 hb[HD]; bf16x8 w[2][5]; f32x4 acc[5]; };
template <int T> DI void g2_step(G2Regs& r, lds_ptr pw, int lane, srd_t sws, unsigned soh) {
    constexpr int nx = T + 1;
    if constexpr (T % 3 == 2 && T != 5 && T < 26) { LBAR(); SB(); }
    wait_h<(T + HD <= 27 ? HD - 1 : 26 - T)>(r.hb[T % HD]); SB();
#pragma unroll
    for (int j = 0; j < 5; ++j) {
        r.acc[j] = __builtin_amdgcn_mfma_f32_16x16x32_bf16(r.w[T & 1][j], __builtin_bit_cast(bf16x8, r.hb[T % HD]), r.acc[j], 0, 0, 0);
        if constexpr (nx < 27) r.w[nx & 1][j] = *(LAS bf16x8*)(pw + g2_slot(nx / 3) + ((nx % 3) * 5 + j) * 1024);
        SB();
    }
    if constexpr (T + HD < 27) { hload(r.hb[T % HD], lane * 16, sws, soh + (T + HD) * 4096); SB(); }
}
template <int B, int... T> DI void g2_steps(G2Regs& r, lds_ptr pw, int lane, srd_t sws, unsigned soh, std::integer_sequence<int, T...>) { (g2_step<B + T>(r, pw, lane, sws, soh), ...); }

__global__ __launch_bounds__(768, 3) void fused_kernel(Params p) {
    extern __shared__ __attribute__((aligned(16))) char smem[];
    lds_ptr lds = (lds_ptr)smem;
    const unsigned lds0 = (unsigned)(uintptr_t)lds;
    const int tid = threadIdx.x, lane = tid & 63, w = __builtin_amdgcn_readfirstlane(tid >> 6);
    const int xg = blockIdx.x & 7, bi = blockIdx.x >> 3;
    const bool gemm_wg = bi >= 5;
    syp sy = (syp)(p.ws + WS_SYNC);
    const srd_t sws = mk_srd(p.ws, WS_END);
    STAMP_DECL; STAMP(0);
    const int rt = 4 * xg + (bi & 3), dt = bi >> 2;
    const unsigned vl16 = lane * 16;
    auto w2_src = [&](int st, int pc) { return WS_WD + (unsigned)((dt * 54 + 3 * st) * 5 + pc + (pc >= 15 ? 27 * 5 - 15 : 0)) * 1024; };
    auto w2_stage = [&](int st, int first, int step, int np) {
#pragma unroll
        for (int i = 0; i < np; ++i) { const int pc = first + step * i; dma16_dev(lds0 + g2_slot(st) + pc * 1024, vl16, sws, w2_src(st, pc)); } };
#define LOAD_E(v) asm volatile("global_load_dword %0, %1, %2 sc1" : "=v"(v) : "v"(0u), "s"(p.ws + WS_SYNC + 128 * SY_E) : "memory")
#define TAKE_TAG(v, N) do { asm volatile("s_waitcnt vmcnt(" #N ")\n\tv_readfirstlane_b32 %0, %1\n\ts_nop 4" : "=s"(tag) : "v"(v) : "memory"); tag += 1u; } while (0)
#define READ_TAG() (tag = __builtin_amdgcn_readfirstlane(lds_ld(lds + AUX_TAG)))
    unsigned tag;
    if (gemm_wg) {
        const int t = (xg & 3) * 27 + (bi - 5), panel = (xg >> 2) * 4 + (t & 3), ft = t >> 2;
        const int ch0 = 80 * xg + 3 * (bi - 5), nch = bi == 31 ? 2 : 3;
        if (w >= 8) {
            const int lw = w - 8;
            const srd_t sx = mk_srd(p.x, L_ * D_ * 2), sg = mk_srd(p.wg, D_ * F_ * 2), su = mk_srd(p.wu, D_ * F_ * 2);
            const unsigned vx0 = (lane >> 3) * 1280 + 16 * ((lane & 7) ^ (lane >> 4)), vx1 = vx0 ^ 64;
            const unsigned vw0 = (lane >> 3) * 3456 + 16 * ((lane & 7) ^ (2 * ((lane >> 4) & 1))), vw1 = vw0 ^ 64;
            const unsigned sox = (unsigned)(panel * 256 + 64 * lw) * 1280, sow = (unsigned)(16 * lw) * 3456 + ft * 128;
            const int rho = ROT1 ? ((xg & 3) * 5 + 1) / 2 + 5 * (xg >> 2) : 0;
            auto tile = [&](int k0, unsigned slot) {
                int kt = k0 + rho; kt -= kt >= 20 ? 20 : kt >= 10 ? 10 : 0;
                dma16(lds0 + slot + G1_WG + lw * 2048, vw0, sg, sow + kt * 64 * 3456);
                dma16(lds0 + slot + G1_WU + lw * 2048, vw0, su, sow + kt * 64 * 3456);
                dma16(lds0 + slot + G1_WG + lw * 2048 + 1024, vw1, sg, sow + kt * 64 * 3456 + 8 * 3456);
                dma16(lds0 + slot + G1_WU + lw * 2048 + 1024, vw1, su, sow + kt * 64 * 3456 + 8 * 3456);
#pragma unroll
                for (int i = 0; i < 8; ++i) dma16(lds0 + slot + (8 * lw + i) * 1024, (i & 1) ? vx1 : vx0, sx, sox + i * 8 * 1280 + kt * 128);
            };
            unsigned e_raw;
            LOAD_E(e_raw);
            asm volatile("s_nop 4" ::: "memory");
            tile(0, 0); tile(1, G1_SLOT);
            TAKE_TAG(e_raw, 12);
            if (lw == 0 && lane == 0) lds_st(lds + AUX_TAG, tag);
            LBAR();
            if (lw == 0) {
                if (lane < 8) __hip_atomic_store((GAS unsigned long long*)(p.ws + WS_STATS) + 8 * blockIdx.x + lane, ((unsigned long long)tag << 32) | lds_ld(lds + AUX_RS + 4 * lane), __ATOMIC_RELAXED, __HIP_MEMORY_SCOPE_AGENT);
                if (lane == 0) sy_st(sy + SY_ACK + blockIdx.x, tag);
            }
            tile(2, 2 * G1_SLOT);
            unsigned sl = 0;
#pragma unroll 1
            for (int kt = 0; kt < 6; ++kt) { WAIT_BAR(12); tile(kt + 3, sl); sl = (sl == 2 * G1_SLOT) ? 0 : sl + G1_SLOT; }
            WAIT_BAR(12);
            const int ch = ch0 + lw;
            const bool cv = lw < nch;
            u32x4 pk0, pk1; unsigned pf;
            { const unsigned vo = WS_STATS + panel * 2048 + (lw == 2 ? lane * 32 : 0);
              hload(pk0, vo, sws, 0u); hload(pk1, vo + 16, sws, 0u); }
            const unsigned vpf = 128u * (lane >= 32 && cv ? SY_K + ch : SY_ALL + xg);
            asm volatile("buffer_load_dword %0, %1, %2, %3 offen sc1" : "=v"(pf) : "v"(vpf), "s"(sws), "s"(WS_SYNC) : "memory");
            tile(9, 0);
            asm volatile("s_waitcnt vmcnt(12) lgkmcnt(0)\n\ts_barrier" : "+v"(pk0), "+v"(pk1), "+v"(pf) :: "memory");
            if (lw == 3) {
                if (!__all(pf == tag)) asm volatile("buffer_load_dword %0, %1, %2, %3 offen sc1" : "=v"(pf) : "v"(vpf), "s"(sws), "s"(WS_SYNC) : "memory");
                asm volatile("s_waitcnt vmcnt(0)" : "+v"(pf) :: "memory");
                const bool wd_ready = __all(pf == tag);
                LBAR();
                if (wd_ready) w2_stage(0, 0, 1, 30);
                LBAR();
                STAMP(1);
                if (!wd_ready) { poll_all<1, 4>(sy + SY_ALL + xg, 1, tag, lane); w2_stage(0, 0, 1, 30); }
                if (WMODE == 0) lds_wait(lds + AUX_DONE, 8, 1, lane);
                if (WMODE == 3) { if (lane == 0) lds_st(lds + AUX_KC + 12, 1u); lds_wait(lds + AUX_KC, 4, 1, lane); }
                w2_stage(1, 0, 1, 30);
                WAIT_VM(30);
                if (WMODE == 1) lds_wait(lds + AUX_DONE, 8, 1, lane);
                if (lane == 0) lds_st(lds + AUX_GO + 8, 1u);
                w2_stage(2, 0, 1, 30);
                STAMP(2);
            } else {
                bool ready = cv && !NOCONV && __all(pf == tag), loaded = false;
                ConvIn c;
                if (!ready) asm volatile("buffer_load_dword %0, %1, %2, %3 offen sc1" : "=v"(pf) : "v"(vpf), "s"(sws), "s"(WS_SYNC) : "memory");
                if (lw == 2) { unsigned g[4] = {pk0[0], pk0[2], pk1[0], pk1[2]};
                    if (!__all(pk0[1] == tag && pk0[3] == tag && pk1[1] == tag && pk1[3] == tag)) stats_poll<4>(g, sws, lane * 32, WS_STATS + panel * 2048, tag);
                    *(LAS u32x4*)(lds + AUX_ST + lane * 16) = u32x4{g[0], g[1], g[2], g[3]}; }
                asm volatile("s_waitcnt vmcnt(0) lgkmcnt(0)\n\ts_barrier" : "+v"(pf) :: "memory");
                ready = cv && !NOCONV && __all(pf == tag);
                if (ready) { conv_load(c, ch, lane, sws); loaded = true; }
                LBAR();
                STAMP(1);
                if (WMODE == 3 && (!cv || NOCONV) && lane == 0) lds_st(lds + AUX_KC + 4 * lw, 1u);
                if (cv && !NOCONV) {
                    lds_ptr cs = lds + lw * CV_BYTES;
                    if (!loaded) {
                        if (WMODE == 3 && lane == 0) lds_st(lds + AUX_KC + 4 * lw, 1u);
#pragma unroll 1
                        for (int it = 0; it < (1 << 18); ++it) { if (__all(sy_ld(sy + (vpf >> 7)) == tag)) break; __builtin_amdgcn_s_sleep(SL_K); }
                        asm volatile("" ::: "memory");
                        conv_load(c, ch, lane, sws); }
                    STAMP(2);
                    __builtin_amdgcn_s_setprio(2);
                    conv_stage_k(c, lane, cs);
                    conv_stage_u(c, lane, cs);
                    if (WMODE == 3 && lane == 0) lds_st(lds + AUX_KC + 4 * lw, 1u);
                    __builtin_amdgcn_s_setprio(0);
                    STAMP(3);
                    conv_run(ch, __uint_as_float(c.inv), lane, cs, sws);
                    STAMP(4);
                }
                if (cv) publish(sy + SY_S + ch, tag, lane);
                STAMP(5);
            }
        } else {
            if (tid < 16 && tid != 1) *(LAS unsigned*)(lds + AUX_W + 4 * tid) = 0u;
            { RowIn a; row_load(a, p.x, 8 * blockIdx.x + w, lane); const unsigned rstat = row_stats(a); if (lane == 0) lds_st(lds + AUX_RS + 4 * w, rstat); }
            const int tg = w >> 1, fg = w & 1, kq = lane >> 4, l16 = lane & 15;
            unsigned ax[2], aw[2];
            { const int row = 64 * tg + l16, q = l16 >> 2, pp = lane & 3;
#pragma unroll
              for (int i = 0; i < 2; ++i) { const int ks = i ^ (tg >> 1);
                  ax[i] = row * 128 + 16 * ((4 * ks + kq) ^ ((row >> 1) & 7));
                  aw[i] = G1_WG + (32 * ks + 8 * kq + q) * 128 + 32 * ((2 * fg + (tg & 1)) ^ ((q >> 1) | ((kq & 1) << 1))) + 8 * pp; } }
            float csg = 0.f, csu = 0.f;
            f32x4 ag[4][2] = {}, au[4][2] = {};
            Frag A, B;
#define RX(N, sl, ks, i) N.x[i] = *(LAS u32x4*)(lds + (sl) + ax[ks] + (i) * 2048)
#define RWL(N, sl, ks, j) N.wl[j] = tr_read(lds + (sl) + (aw[ks] ^ (((j) & 1) * 32)) + ((j) >> 1) * 8192)
#define RWH(N, sl, ks, j) N.wh[j] = tr_read(lds + (sl) + (aw[ks] ^ (((j) & 1) * 32)) + ((j) >> 1) * 8192 + 512)
#define MG(C, i, j) MF16(ag[i][j], C.wl[j], C.wh[j], C.x[i])
#define MU(C, i, j) MF16(au[i][j], C.wl[2 + j], C.wh[2 + j], C.x[i])
#define G_(M, RD, R) do { M; if (RD) { R; } SB(); } while (0)
#define CSUM(acc, v, k) do { const unsigned d_ = __builtin_bit_cast(u32x2, v)[k]; acc += bflo(d_); acc2 += bfhi(d_); asm volatile("" : "+v"(acc), "+v"(acc2)); } while (0)
#define STEPC(C, N, RD, sl, ks) do { \
        { float acc2 = 0.f; \
        G_(MG(C, 0, 0), RD, RX(N, sl, ks, 0));  G_(MU(C, 0, 0); CSUM(csg, C.wl[0], 0), RD, RWL(N, sl, ks, 0)); G_(MG(C, 0, 1), RD, RWH(N, sl, ks, 0)); G_(MU(C, 0, 1); CSUM(csg, C.wl[0], 1), RD, RWL(N, sl, ks, 2)); \
        G_(MG(C, 1, 0), RD, RWH(N, sl, ks, 2)); G_(MU(C, 1, 0); CSUM(csg, C.wh[0], 0), RD, RWL(N, sl, ks, 1)); G_(MG(C, 1, 1), RD, RWH(N, sl, ks, 1)); G_(MU(C, 1, 1); CSUM(csg, C.wh[0], 1), RD, RWL(N, sl, ks, 3)); \
        csg += acc2; } { float acc2 = 0.f; \
        G_(MG(C, 2, 0), RD, RWH(N, sl, ks, 3)); G_(MU(C, 2, 0); CSUM(csu, C.wl[2], 0), RD, RX(N, sl, ks, 1));  G_(MG(C, 2, 1), RD, RX(N, sl, ks, 2));  G_(MU(C, 2, 1); CSUM(csu, C.wl[2], 1), RD, RX(N, sl, ks, 3)); \
        G_(MG(C, 3, 0), 0, (void)0); G_(MU(C, 3, 0); CSUM(csu, C.wh[2], 0), 0, (void)0); G_(MG(C, 3, 1), 0, (void)0); G_(MU(C, 3, 1); CSUM(csu, C.wh[2], 1), 0, (void)0); \
        csu += acc2; } } while (0)
#define STEP(C, N, RD, sl, ks) do { \
        G_(MG(C, 0, 0), RD, RX(N, sl, ks, 0));  G_(MU(C, 0, 0), RD, RWL(N, sl, ks, 0)); G_(MG(C, 0, 1), RD, RWH(N, sl, ks, 0)); G_(MU(C, 0, 1), RD, RWL(N, sl, ks, 2)); \
        G_(MG(C, 1, 0), RD, RWH(N, sl, ks, 2)); G_(MU(C, 1, 0), RD, RWL(N, sl, ks, 1)); G_(MG(C, 1, 1), RD, RWH(N, sl, ks, 1)); G_(MU(C, 1, 1), RD, RWL(N, sl, ks, 3)); \
        G_(MG(C, 2, 0), RD, RWH(N, sl, ks, 3)); G_(MU(C, 2, 0), RD, RX(N, sl, ks, 1));  G_(MG(C, 2, 1), RD, RX(N, sl, ks, 2));  G_(MU(C, 2, 1), RD, RX(N, sl, ks, 3)); \
        G_(MG(C, 3, 0), 0, (void)0); G_(MU(C, 3, 0), 0, (void)0); G_(MG(C, 3, 1), 0, (void)0); G_(MU(C, 3, 1), 0, (void)0); } while (0)
            LBAR();
            READ_TAG();
            STAMP(1);
            RX(A, 0, 0, 0); RWL(A, 0, 0, 0); RWH(A, 0, 0, 0); RWL(A, 0, 0, 2); RWH(A, 0, 0, 2); RWL(A, 0, 0, 1); RWH(A, 0, 0, 1); RWL(A, 0, 0, 3); RWH(A, 0, 0, 3);
            RX(A, 0, 0, 1); RX(A, 0, 0, 2); RX(A, 0, 0, 3);
#pragma unroll
            for (int kt = 0; kt < 9; ++kt) {
                if (NOCS) STEP(A, B, 1, (kt % 3) * G1_SLOT, 1); else STEPC(A, B, 1, (kt % 3) * G1_SLOT, 1);
                LBAR(); SB();
                STEP(B, A, 1, ((kt + 1) % 3) * G1_SLOT, 0);
            }
            STEPC(A, B, 1, 0, 1);
            { auto a = __builtin_amdgcn_permlane16_swap(__float_as_uint(csg), __float_as_uint(csu), false, false);
              const float v = __uint_as_float(a[0]) + __uint_as_float(a[1]);
              auto b = __builtin_amdgcn_permlane32_swap(__float_as_uint(v), __float_as_uint(v), false, false);
              if (lane < 32) *(LAS float*)(lds + AUX_CS + (tg >> 1) * 512 + (lane >> 4) * 256 + (32 * fg + 16 * (tg & 1) + l16) * 4) = __uint_as_float(b[0]) + __uint_as_float(b[1]); }
            SB();
            STEP(B, A, 0, 0, 0);
            STAMP(2);
            LBAR();
            __builtin_amdgcn_s_setprio(1);
            f32x4 cgv[2], cuv[2];
#pragma unroll
            for (int j = 0; j < 2; ++j) { lds_ptr q = lds + AUX_CS + (32 * fg + 16 * (j ^ (tg & 1)) + 4 * kq) * 4;
                cgv[j] = *(LAS f32x4*)q + *(LAS f32x4*)(q + 512); cuv[j] = *(LAS f32x4*)(q + 256) + *(LAS f32x4*)(q + 768); }
            unsigned stt[4];
#pragma unroll
            for (int i = 0; i < 4; ++i) stt[i] = *(LAS unsigned*)(lds + AUX_ST + (64 * tg + 16 * i + l16) * 4);
            const float K0 = -2.f * 0.7978845608028654f * 1.44269504088896341f, K1 = K0 * 0.044715f;
#pragma unroll
            for (int i = 0; i < 4; ++i) {
                const float ir = bfhi(stt[i]), r = __builtin_amdgcn_rcpf(ir);
                const f32x2 nm = {-bflo(stt[i]), -bflo(stt[i])}, c0 = {K0 * r, K0 * r}, c1 = {K1 * r * r * r, K1 * r * r * r}, ir2 = {ir * ir, ir * ir};
                u32x4 o;
#pragma unroll
                for (int e = 0; e < 4; ++e) {
                    const int j = e >> 1, b = 2 * (e & 1);
                    const f32x2 g1 = nm * f32x2{cgv[j][b], cgv[j][b + 1]} + f32x2{ag[i][j][b], ag[i][j][b + 1]};
                    const f32x2 u1 = nm * f32x2{cuv[j][b], cuv[j][b + 1]} + f32x2{au[i][j][b], au[i][j][b + 1]};
                    const f32x2 z = g1 * (g1 * g1 * c1 + c0);
                    const f32x2 ex = {__builtin_amdgcn_exp2f(z[0]), __builtin_amdgcn_exp2f(z[1])};
                    const f32x2 dd = ex * ir2 + ir2;
                    const f32x2 hv = g1 * u1 * f32x2{__builtin_amdgcn_rcpf(dd[0]), __builtin_amdgcn_rcpf(dd[1])};
                    o[e] = pk_bf16(hv[0], hv[1]);
                }
                if (tg & 1) o = u32x4{o[2], o[3], o[0], o[1]};
                st16_dev(o, sws, vl16, WS_H + (unsigned)(((panel * 4 + tg) * 54 + ft * 2 + fg) * 4 + i) * 1024);
            }
            if (WMODE < 2 && lane == 0) lds_st(lds + AUX_DONE + 4 * w, 1u);
            STAMP(3);
            publish(sy + SY_H + 64 * (4 * panel + tg) + 2 * ft + fg, tag, lane);
            STAMP(4);
        }
    } else {
        const int hi = bi * 8 + xg;
        unsigned e_raw;
        if (tid < 16) *(LAS unsigned*)(lds + AUX_W + 4 * tid) = 0u;
        LBAR();
        if (w < 8) {
            const int d0 = 8 * hi + w, d1 = d0 + 320;
            KgIn k0, k1; RowIn a;
            LOAD_E(e_raw);
            row_load(a, p.x, 8 * blockIdx.x + w, lane);
            kgen_load(k0, p, d0, lane);
            kgen_load(k1, p, d1, lane);
            const unsigned rstat = row_stats(a);
            TAKE_TAG(e_raw, 8);
            row_publish(rstat, 8 * blockIdx.x + w, lane, tag, p.ws);
            if (lane == 0) lds_st(lds + AUX_DONE + 4 * w, 1u);
            STAMP(1);
#pragma unroll 1
            for (int i = 0; i < NKG; ++i) {
                kgen_channel(k0, i ? d1 : d0, lane, lds + HK_BASE + w * KG_BYTES, lds + HK_BASE + 8 * KG_BYTES + w * KT_BYTES, sws, i ? sy + SY_K + d0 : nullptr, tag);
                if (i == 0) STAMP(2);
                k0 = k1;
            }
            STAMP(3);
            publish(sy + SY_K + d1, tag, lane);
            if (NKG < 2 && lane == 0) sy_st(sy + SY_K + d0, tag);
            STAMP(4);
        } else {
            const int aw = hi * 4 + (w - 8), off = (w - 8) * 16384;
            const srd_t swd = mk_srd(p.wd, D_ * F_ * 2);
            const bool two = aw + 160 < 270;
            LOAD_E(e_raw);
            lds_wait(lds + AUX_DONE, 8, 1, lane);
            __builtin_amdgcn_s_setprio(HPRIO);
            asm volatile("s_nop 4" ::: "memory");
            wd_issue(aw, lane, lds0 + off, swd);
            wd_issue(two ? aw + 160 : aw, lane, lds0 + off + 8192, swd);
            TAKE_TAG(e_raw, 0);
            STAMP(1);
            wd_finish(aw, lane, lds + off, sws);
            if (two) wd_finish(aw + 160, lane, lds + off + 8192, sws);
            lnt_issue(aw, lane, lds0 + off, mk_srd(p.x, L_ * D_ * 2));
            STAMP(2);
            unsigned g[8];
            stats_poll<8>(g, sws, (2 * (lane >> 4) + ((lane >> 3) & 1)) * 64, WS_STATS + (aw & 31) * 512, tag);
            WAIT_VM(0);
            STAMP(3);
            lnt_finish(aw, g, lane, lds + off, sws);
            STAMP(6);
            publish(sy + SY_LN + aw, tag, lane);
            STAMP(4);
            if (w == 8 && bi == 0) { poll_all<3, 4>(sy + SY_LN, 160, tag, lane); if (lane == 0) sy_st(sy + SY_ALL + xg, tag); }
            if (w == 11) poll_all<1, 16>(sy + SY_ALL + xg, 1, tag, lane);
            __builtin_amdgcn_s_setprio(0);
        }
        LBAR();
        if (w == 9 && lane == 0) sy_st(sy + SY_ACK + blockIdx.x, tag);
        if (w == 10 && hi == 39) { poll_all<4, 64>(sy + SY_ACK, 256, tag, lane); if (lane == 0) sy_st(sy + SY_E, tag); }
        if (w == 11) { w2_stage(0, 0, 1, 30); w2_stage(1, 0, 1, 30); WAIT_VM(30); if (lane == 0) lds_st(lds + AUX_GO + 8, 1u); w2_stage(2, 0, 1, 30); }
    }
    if (w >= 8) {
        const int lw = w - 8;
        WAIT_BAR(0);
        STAMP(6);
        if (lw > 0) {
            w2_stage(3, lw - 1, 3, 10); w2_stage(4, lw - 1, 3, 10);
            WAIT_BAR(10); w2_stage(5, lw - 1, 3, 10);
            WAIT_BAR(10); w2_stage(6, lw - 1, 3, 10);
            WAIT_BAR(10); w2_stage(7, lw - 1, 3, 10);
            WAIT_BAR(10); w2_stage(8, lw - 1, 3, 10);
            WAIT_BAR(10); WAIT_BAR(0);
        } else {
            const srd_t sx = mk_srd(p.x, L_ * D_ * 2);
            unsigned f0, f1;
            LBAR(); LBAR();
            asm volatile("buffer_load_dword %0, %1, %2, %3 offen sc1" : "=v"(f0) : "v"(128u * lane), "s"(sws), "s"(WS_SYNC + 128u * (SY_S + 80 * dt)) : "memory");
            asm volatile("buffer_load_dword %0, %1, %2, %3 offen sc1" : "=v"(f1) : "v"(128u * (64 + (lane & 15))), "s"(sws), "s"(WS_SYNC + 128u * (SY_S + 80 * dt)) : "memory");
            LBAR();
#pragma unroll
            for (int i = 0; i < 10; ++i) { const unsigned o = i * 64 + lane; dma16(lds0 + G2_X + i * 1024, (o / 10) * 1280 + (o % 10) * 16, sx, (unsigned)(rt * 64 * 1280 + dt * 160)); }
            asm volatile("s_waitcnt vmcnt(10)" : "+v"(f0), "+v"(f1) :: "memory");
            const bool s_ready = __all(f0 == tag && f1 == tag);
            auto s_tile = [&]() {
#pragma unroll
                for (int i = 0; i < 10; ++i) dma16_dev(lds0 + G2_S + i * 1024, vl16, sws, WS_ST + (unsigned)(rt * D_ + 80 * dt) * 128 + i * 1024); };
            if (s_ready) s_tile();
            LBAR(); LBAR(); LBAR();
            if (!s_ready) { poll_all<2, 2>(sy + SY_S + 80 * dt, 80, tag, lane); s_tile(); }
            STAMP(7);
            WAIT_VM(0);
        }
        LBAR();
        STAMP(8);
    } else {
        const int tt = w & 3, kh = w >> 2, q = lane >> 4;
        const unsigned soh = WS_H + (unsigned)((rt * 54 + 27 * kh) * 4 + tt) * 1024;
        __builtin_amdgcn_s_setprio(3);
        if (w == 0) { if (gemm_wg) poll_all<1, SL_H>(sy + SY_H + 64 * rt, 54, tag, lane); else poll_all<1, SL_HH>(sy + SY_H + 64 * rt, 54, tag, lane);
                      if (lane == 0) lds_st(lds + AUX_GO, 1u); }
        else lds_wait(lds + AUX_GO, 1, 1, lane);
        STAMP(5);
        G2Regs r;
        lds_ptr pw = lds + kh * G2_HALF + vl16;
#pragma unroll
        for (int k = 0; k < HD; ++k) { hload(r.hb[k], vl16, sws, soh + k * 4096); if (k == 1) __builtin_amdgcn_s_setprio(1); }
        lds_wait(lds + AUX_GO + 8, 1, 1, lane);
#pragma unroll
        for (int j = 0; j < 5; ++j) { r.acc[j] = f32x4{0.f, 0.f, 0.f, 0.f}; r.w[0][j] = *(LAS bf16x8*)(pw + g2_slot(0) + j * 1024); }
        SB();
        g2_steps<0>(r, pw, lane, sws, soh, std::make_integer_sequence<int, 2>{});
        G2STAMP(6);
        g2_steps<2>(r, pw, lane, sws, soh, std::make_integer_sequence<int, 1>{});
        G2STAMP(7);
        g2_steps<3>(r, pw, lane, sws, soh, std::make_integer_sequence<int, 6>{});
        G2STAMP(11);
        g2_steps<9>(r, pw, lane, sws, soh, std::make_integer_sequence<int, 9>{});
        G2STAMP(13);
        g2_steps<18>(r, pw, lane, sws, soh, std::make_integer_sequence<int, 9>{});
        STAMP(8);
        lds_ptr px = lds + g2_slot(5) + tt * 5120 + vl16;
        if (kh) { *(LAS f32x4*)(px) = r.acc[0]; *(LAS f32x4*)(px + 1024) = r.acc[1]; *(LAS f32x4*)(px + 2048) = r.acc[2]; }
        else    { *(LAS f32x4*)(px + 3072) = r.acc[3]; *(LAS f32x4*)(px + 4096) = r.acc[4]; }
        LBAR();
        STAMP(9);
        auto fin = [&](int j, f32x4 a) {
            const int ro = (tt * 16 + (lane & 15)) * 160 + 32 * j + 8 * q;
            const u32x2 xx = *(LAS u32x2*)(lds + G2_X + ro);
            a += *(LAS f32x4*)(px + j * 1024);
            const s16x4 sv = tr_read(lds + G2_S + (16 * j + 4 * q + ((lane & 15) >> 2)) * 128 + tt * 32 + (lane & 3) * 8);
            float o[4];
#pragma unroll
            for (int i = 0; i < 4; ++i) {
                const float sg = a[i] * __builtin_amdgcn_rcpf(1.f + __builtin_amdgcn_exp2f(-1.44269504088896341f * a[i]));
                o[i] = fmaf(bf2f((unsigned short)sv[i]), sg, (i & 1) ? bfhi(xx[i >> 1]) : bflo(xx[i >> 1]));
            }
            *(LAS u32x2*)(lds + G2_O + ro) = u32x2{pk_bf16(o[0], o[1]), pk_bf16(o[2], o[3])};
        };
        if (kh == 0) { fin(0, r.acc[0]); fin(1, r.acc[1]); fin(2, r.acc[2]); } else { fin(3, r.acc[3]); fin(4, r.acc[4]); }
    }
    LBAR();
    if (w < 10) *(u32x4*)(p.out + (size_t)(rt * 64 + tid / 10) * D_ + 80 * dt + (tid % 10) * 8) = *(LAS u32x4*)(lds + G2_O + tid * 16);
    STAMP(10);
    STAMP_FLUSH(p.ws, (gemm_wg ? STWG : STWH));
}

extern "C" void kernel_launch(void* const* d_in, const int* in_sizes, int n_in, void* d_out, int out_size, void* d_ws, size_t ws_size,
                              hipStream_t stream) {
    Params p;
    p.x = (cu16p)d_in[0]; p.lar = (cu16p)d_in[1]; p.aim = (cu16p)d_in[2]; p.cre = (cu16p)d_in[3]; p.cim = (cu16p)d_in[4];
    p.ldt = (cu16p)d_in[5]; p.dsk = (cu16p)d_in[6]; p.wg = (cu16p)d_in[8]; p.wu = (cu16p)d_in[9]; p.wd = (cu16p)d_in[10];
    p.out = (unsigned short*)d_out; p.ws = (char*)d_ws;
    (void)hipFuncSetAttribute((const void*)fused_kernel, hipFuncAttributeMaxDynamicSharedMemorySize, LDS_BYTES);
    fused_kernel<<<256, 768, LDS_BYTES, stream>>>(p);
}
```

```cpp
#include <hip/hip_runtime.h>
#include <hip/hip_bf16.h>
#include <stdint.h>
#include <type_traits>
#include <utility>

#define DI __device__ __forceinline__
#define LAS __attribute__((address_space(3)))
#define GAS __attribute__((address_space(1)))
typedef short s16x8 __attribute__((ext_vector_type(8)));
typedef short s16x4 __attribute__((ext_vector_type(4)));
typedef __bf16 bf16x8 __attribute__((ext_vector_type(8)));
typedef __bf16 bf16x2 __attribute__((ext_vector_type(2)));
typedef _Float16 f16x8 __attribute__((ext_vector_type(8)));
typedef _Float16 f16x2 __attribute__((ext_vector_type(2)));
typedef float f32x16 __attribute__((ext_vector_type(16)));
typedef float f32x4 __attribute__((ext_vector_type(4)));
typedef float f32x2 __attribute__((ext_vector_type(2)));
typedef unsigned u32x4 __attribute__((ext_vector_type(4)));
typedef unsigned u32x2 __attribute__((ext_vector_type(2)));
typedef LAS char* lds_ptr;
typedef const unsigned short* cu16p;
typedef __amdgpu_buffer_rsrc_t srd_t;

constexpr int L_ = 2048, D_ = 640, N_ = 128, F_ = 1728;
constexpr unsigned WS_SYNC = 0;
constexpr unsigned WS_STATS = 512u << 10;
constexpr unsigned WS_KT = 576u << 10;
constexpr unsigned WS_H = 2u << 20;
constexpr unsigned WS_UT = 9u << 20;
constexpr unsigned WS_ST = 12u << 20;
constexpr unsigned WS_WD = 15u << 20;
constexpr unsigned WS_END = 18u << 20;
constexpr size_t WS_STAMP = (size_t)32 << 20;
constexpr int SY_E = 0, SY_ACK = 1, SY_LN = 257, SY_WD = 417, SY_ALL = 687, SY_S = 695, SY_K = 1335, SY_H = 1975;
static_assert((SY_H + 2048) * 128 <= WS_STATS, "sync map");
constexpr int SC1 = 16;
constexpr int LDS_BYTES = 163840;

#define STAMP_DECL do {} while (0)
#define STAMP(i) do {} while (0)
#define STAMP_FLUSH(ws, wv) do {} while (0)
#define TOUCH 1
#define NOCS 0
#define G2STAMP(i) STAMP(i)
#define NOHELP 0
#define HSLEEP 6
#define SL_ST 12
#define SL_K 3
#define SL_S 16
#define SL_H 1
#define SL_HH 16
#define ROT1 1
#define ROT2 0
#define FULLP 22
#define WNT 1
#define WMODE 3
#define HD 4
#define WLAG 4
#define GSLEEP 2
#define PNT 1
#define WSLEEP 0
#define WF1 10
#define T1HOLD 0
#define W3EARLY 1
#define HPRIO 1
#define STDELAY 6000
#define NKG 2
#define NOCONV 0
#define WLATE 0
#define SB() __builtin_amdgcn_sched_barrier(0)

DI float bf2f(unsigned short v) { return __uint_as_float((unsigned)v << 16); }
DI float bflo(unsigned v) { return __uint_as_float(v << 16); }
DI float bfhi(unsigned v) { return __uint_as_float(v & 0xffff0000u); }
DI float rbf(float x) { return (float)(__bf16)x; }
DI unsigned pk_bf16(float lo, float hi) { bf16x2 v = {(__bf16)lo, (__bf16)hi}; return __builtin_bit_cast(unsigned, v); }
DI unsigned pk_f16(float lo, float hi) { f16x2 v = {(_Float16)lo, (_Float16)hi}; return __builtin_bit_cast(unsigned, v); }
DI s16x4 tr_read(lds_ptr p) { return __builtin_amdgcn_ds_read_tr16_b64_v4i16((LAS s16x4*)p); }
DI srd_t mk_srd(const void* p, unsigned bytes) { return __builtin_amdgcn_make_buffer_rsrc((void*)p, (short)0, (int)bytes, 0x00020000); }
DI void dma16(unsigned ldsaddr, unsigned voff, srd_t srd, unsigned soff) {
    asm volatile("s_mov_b32 m0, %0\n\ts_nop 0\n\tbuffer_load_dwordx4 %1, %2, %3 offen lds" :: "s"(ldsaddr), "v"(voff), "s"(srd), "s"(soff) : "memory");
}
DI void dma16_nt(unsigned ldsaddr, unsigned voff, srd_t srd, unsigned soff) {
    if (WNT) asm volatile("s_mov_b32 m0, %0\n\ts_nop 0\n\tbuffer_load_dwordx4 %1, %2, %3 offen nt lds" :: "s"(ldsaddr), "v"(voff), "s"(srd), "s"(soff) : "memory");
    else dma16(ldsaddr, voff, srd, soff);
}
DI void dma16_dev(unsigned ldsaddr, unsigned voff, srd_t srd, unsigned soff) {
    asm volatile("s_mov_b32 m0, %0\n\ts_nop 0\n\tbuffer_load_dwordx4 %1, %2, %3 offen sc1 lds" :: "s"(ldsaddr), "v"(voff), "s"(srd), "s"(soff) : "memory");
}
DI void hload(u32x4& dst, unsigned vo, srd_t srd, unsigned so) { asm volatile("buffer_load_dwordx4 %0, %1, %2, %3 offen sc1" : "=v"(dst) : "v"(vo), "s"(srd), "s"(so) : "memory"); }
DI u32x4 ld16_dev(srd_t s, unsigned vo, unsigned so) { return __builtin_amdgcn_raw_buffer_load_b128(s, vo, so, SC1); }
DI u32x2 ld8_dev(srd_t s, unsigned vo, unsigned so) { return __builtin_amdgcn_raw_buffer_load_b64(s, vo, so, SC1); }
DI void st16_dev(u32x4 v, srd_t s, unsigned vo, unsigned so) {
    asm volatile("buffer_store_dwordx4 %0, %1, %2, %3 offen sc1\n\ts_nop 1" :: "v"(v), "v"(vo), "s"(s), "s"(so)); }
DI void st8_dev(u32x2 v, srd_t s, unsigned vo, unsigned so) { __builtin_amdgcn_raw_buffer_store_b64(v, s, vo, so, SC1); }
#define WAIT_BAR(N) asm volatile("s_waitcnt vmcnt(" #N ") lgkmcnt(0)\n\ts_barrier" ::: "memory")
#define LBAR() asm volatile("s_waitcnt lgkmcnt(0)\n\ts_barrier" ::: "memory")
#define WAIT_VM(N) asm volatile("s_waitcnt vmcnt(" #N ")" ::: "memory")
#define WAIT_LG() asm volatile("s_waitcnt lgkmcnt(0)" ::: "memory")
struct alignas(128) SyncLine { unsigned v; };
typedef GAS SyncLine* syp;
DI unsigned sy_ld(syp p) { return __hip_atomic_load(&p->v, __ATOMIC_RELAXED, __HIP_MEMORY_SCOPE_AGENT); }
DI void sy_st(syp p, unsigned v) { __hip_atomic_store(&p->v, v, __ATOMIC_RELAXED, __HIP_MEMORY_SCOPE_AGENT); }
template <int K, int SLP = 2> DI void poll_all(syp p, int n, unsigned tag, int lane) {
    for (int it = 0; it < (1 << 18); ++it) {
        bool ok = true;
#pragma unroll
        for (int k = 0; k < K; ++k) { const int i = lane + 64 * k; ok &= sy_ld(p + (i < n ? i : n - 1)) == tag; }
        if (__all(ok)) break;
        __builtin_amdgcn_s_sleep(SLP);
    }
    asm volatile("" ::: "memory");
}
DI unsigned lds_ld(lds_ptr p) { return __hip_atomic_load((LAS unsigned*)p, __ATOMIC_RELAXED, __HIP_MEMORY_SCOPE_WORKGROUP); }
DI void lds_st(lds_ptr p, unsigned v) { __hip_atomic_store((LAS unsigned*)p, v, __ATOMIC_RELAXED, __HIP_MEMORY_SCOPE_WORKGROUP); }
DI void lds_wait(lds_ptr p, int n, unsigned v, int lane) {
#pragma unroll 1
    for (int it = 0; it < (1 << 22); ++it) {
        if (__all(lds_ld(p + 4 * (lane & (n - 1))) >= v)) break;
        __builtin_amdgcn_s_sleep(1);
    }
    asm volatile("" ::: "memory");
}
DI void publish(syp p, unsigned tag, int lane) { WAIT_VM(0); if (lane == 0) sy_st(p, tag); }
DI float dot2bf(unsigned a, unsigned b, float c) { return __builtin_amdgcn_fdot2_f32_bf16(__builtin_bit_cast(bf16x2, a), __builtin_bit_cast(bf16x2, b), c, false); }

template <int CTRL, int RM> DI float dpp_add(float v) { return v + __builtin_bit_cast(float, __builtin_amdgcn_update_dpp(0, __builtin_bit_cast(int, v), CTRL, RM, 0xf, true)); }
DI float wave_sum(float v) {
    v = dpp_add<0x111, 0xf>(v); v = dpp_add<0x112, 0xf>(v); v = dpp_add<0x114, 0xf>(v); v = dpp_add<0x118, 0xf>(v);
    v = dpp_add<0x142, 0xa>(v); v = dpp_add<0x143, 0xc>(v);
    return __builtin_bit_cast(float, __builtin_amdgcn_readlane(__builtin_bit_cast(int, v), 63));
}
DI float sum_8_16_32(float v) {
    v = dpp_add<0x128, 0xf>(v);
    auto a = __builtin_amdgcn_permlane16_swap(__float_as_uint(v), __float_as_uint(v), false, false); v = __uint_as_float(a[0]) + __uint_as_float(a[1]);
    auto b = __builtin_amdgcn_permlane32_swap(__float_as_uint(v), __float_as_uint(v), false, false); return __uint_as_float(b[0]) + __uint_as_float(b[1]);
}

struct Params { cu16p x, lar, aim, cre, cim, ldt, dsk, wg, wu, wd; unsigned short* out; char* ws; };

struct RowIn { unsigned v[5]; };
DI void row_load(RowIn& a, cu16p x, int row, int lane) {
#pragma unroll
    for (int i = 0; i < 5; ++i) a.v[i] = ((const unsigned*)(x + (size_t)row * D_))[64 * i + lane];
}
DI unsigned row_stats(const RowIn& a) {
    float s = 0.f;
#pragma unroll
    for (int i = 0; i < 5; ++i) s += bflo(a.v[i]) + bfhi(a.v[i]);
    const float mean = wave_sum(s) * (1.f / D_);
    float q = 0.f;
#pragma unroll
    for (int i = 0; i < 5; ++i) { const float u = bflo(a.v[i]) - mean, v = bfhi(a.v[i]) - mean; q += u * u + v * v; }
    const float sd = sqrtf(rbf(rbf(wave_sum(q) * (1.f / D_)) + 1e-5f));
    return pk_bf16(mean, sd);
}
DI void row_publish(unsigned v, int row, int lane, unsigned tag, char* ws) {
    if (lane == 0) __hip_atomic_store((GAS unsigned long long*)(ws + WS_STATS) + row, ((unsigned long long)tag << 32) | v, __ATOMIC_RELAXED, __HIP_MEMORY_SCOPE_AGENT);
}
template <int NG> DI void stats_poll(unsigned (&g)[NG], srd_t sws, unsigned vo, unsigned so, unsigned tag) {
#pragma unroll 1
    for (int it = 0; it < (1 << 18); ++it) {
        bool ok = true;
#pragma unroll
        for (int k = 0; k < NG; ++k) { const u32x2 t = ld8_dev(sws, vo + 8 * k, so); g[k] = t[0]; ok &= t[1] == tag; }
        if (__all(ok)) break;
        __builtin_amdgcn_s_sleep(SL_ST);
    }
}
DI void lnt_issue(int u, int lane, unsigned ldsaddr, srd_t sx) {
    ldsaddr = __builtin_amdgcn_readfirstlane(ldsaddr);
    const int blk = u & 31, cb = u >> 5, r4 = lane >> 4;
#pragma unroll
    for (int i = 0; i < 16; ++i) dma16(ldsaddr + i * 1024, (unsigned)(r4 * 1280 + 16 * ((lane & 15) ^ ((((i >> 1) & 3) << 2) | r4))), sx, (unsigned)((blk * 64 + 4 * i) * 1280 + cb * 256));
}
DI void lnt_finish(int u, const unsigned (&g)[8], int lane, lds_ptr lds, srd_t sws) {
    const int blk = u & 31, cb = u >> 5, gq = lane >> 4, q = (lane & 15) >> 2, pp = lane & 3, oc = 2 * gq + (pp >> 1);
    f32x2 rs[4], nm[4];
#pragma unroll
    for (int e = 0; e < 4; ++e) { rs[e] = f32x2{__builtin_amdgcn_rcpf(bfhi(g[2 * e])), __builtin_amdgcn_rcpf(bfhi(g[2 * e + 1]))}; nm[e] = -f32x2{bflo(g[2 * e]), bflo(g[2 * e + 1])} * rs[e]; }
    const unsigned ra = (8 * oc + q) * 256 + 8 * (pp & 1), fx = 16 * (((oc & 3) << 2) | q);
    lds_ptr wo = lds + (lane & 7) * 128 + 16 * ((2 * gq + ((lane >> 3) & 1)) ^ (lane & 7)), ro = lds + (lane >> 3) * 128 + 16 * ((lane & 7) ^ (lane >> 3));
    u32x2 tl[16], th[16];
#pragma unroll
    for (int k = 0; k < 16; ++k) { tl[k] = __builtin_bit_cast(u32x2, tr_read(lds + ra + ((16 * k) ^ fx))); th[k] = __builtin_bit_cast(u32x2, tr_read(lds + ra + 1024 + ((16 * k) ^ fx))); }
    WAIT_LG();
#pragma unroll
    for (int k = 0; k < 16; ++k) {
        const unsigned in[4] = {tl[k][0], tl[k][1], th[k][0], th[k][1]};
        u32x4 o;
#pragma unroll
        for (int e = 0; e < 4; ++e) { f32x2 t = __builtin_elementwise_fma(f32x2{bflo(in[e]), bfhi(in[e])}, rs[e], nm[e]); asm("" : "+v"(t)); o[e] = pk_f16(t[0], t[1]); }
        *(LAS u32x4*)(wo + k * 1024) = o;
    }
    WAIT_LG();
    u32x4 o[16];
#pragma unroll
    for (int k = 0; k < 16; ++k) o[k] = *(LAS u32x4*)(ro + k * 1024);
#pragma unroll
    for (int k = 0; k < 16; ++k) st16_dev(o[k], sws, lane * 16, WS_UT + (unsigned)(blk * D_ + cb * 128) * 128 + k * 1024);
}

struct CM { f32x2 d, n; };
DI CM cm(f32x2 w) { return CM{f32x2{w[0], w[0]}, f32x2{-w[1], w[1]}}; }
DI f32x2 cmul(f32x2 x, const CM& m) { return __builtin_elementwise_fma(__builtin_shufflevector(x, x, 1, 0), m.n, x * m.d); }
DI f32x2 cexpp(float er, float rev, float pos) {
    const float m = __builtin_amdgcn_exp2f(er * pos);
    const float t = __builtin_amdgcn_fractf(rev * pos);
    return f32x2{m * __builtin_amdgcn_cosf(t), m * __builtin_amdgcn_sinf(t)};
}
constexpr int KG_ROW = 272, KG_BYTES = 32 * KG_ROW, KT_BYTES = 1312, KT_S = 512, KT_INV = 1296;
struct KgIn { unsigned lar, aim, cre, cim, ldt, dsk; };
DI void kgen_load(KgIn& k, const Params& p, int d, int lane) {
    const int n = d * (N_ / 2) + lane;
    if (PNT) { k.lar = __builtin_nontemporal_load((const unsigned*)p.lar + n); k.aim = __builtin_nontemporal_load((const unsigned*)p.aim + n);
               k.cre = __builtin_nontemporal_load((const unsigned*)p.cre + n); k.cim = __builtin_nontemporal_load((const unsigned*)p.cim + n); }
    else { k.lar = ((const unsigned*)p.lar)[n]; k.aim = ((const unsigned*)p.aim)[n]; k.cre = ((const unsigned*)p.cre)[n]; k.cim = ((const unsigned*)p.cim)[n]; }
    const unsigned a = ((const unsigned*)p.ldt)[d >> 1], b = ((const unsigned*)p.dsk)[d >> 1];
    k.ldt = (d & 1) ? a & 0xffff0000u : a << 16; k.dsk = (d & 1) ? b & 0xffff0000u : b << 16;
}
struct KgMode { float er, rv, dt; };
DI KgMode kg_mode(const KgIn& k, int hf) {
    const float dt = rbf(expf(__uint_as_float(k.ldt)));
    return KgMode{(-rbf(expf(hf ? bfhi(k.lar) : bflo(k.lar))) * dt) * 1.44269504088896341f, ((hf ? bfhi(k.aim) : bflo(k.aim)) * dt) * 0.15915494309189535f, dt};
}
#define PUTA(row, g) *(LAS unsigned*)(TW + (row) * KG_ROW) = pk_f16((g)[0], -(g)[1])
#define PUTB(row, g) *(LAS unsigned*)(TW + (row) * KG_ROW) = pk_f16((g)[0], (g)[1])
template <int N> DI void cmuln(f32x2 (&o)[N], const f32x2 (&x)[N], const CM (&m)[N]) {
    f32x2 t[N];
#pragma unroll
    for (int i = 0; i < N; ++i) t[i] = x[i] * m[i].d;
    SB();
#pragma unroll
    for (int i = 0; i < N; ++i) o[i] = __builtin_elementwise_fma(__builtin_shufflevector(x[i], x[i], 1, 0), m[i].n, t[i]);
    SB();
}
DI void kg_e(const KgMode& m, int lane, lds_ptr tab) {
    lds_ptr TW = tab + lane * 4;
    const f32x2 w8 = cexpp(m.er, m.rv, 8.f), w32 = cexpp(m.er, m.rv, 32.f);
    const CM m8 = cm(w8), m32 = cm(w32);
    f32x2 eb[4]; eb[0] = f32x2{1.f, 0.f}; eb[1] = w8; eb[2] = cmul(w8, m8); eb[3] = cmul(eb[2], m8);
    const CM mm[4] = {m32, m32, m32, m32};
    SB();
#pragma unroll
    for (int a = 0; a < 8; ++a) {
        f32x2 nx[4];
        if (a < 7) {
            f32x2 t[4];
#pragma unroll
            for (int b = 0; b < 4; ++b) t[b] = eb[b] * m32.d;
            SB();
            PUTB(4 * a, eb[0]); PUTB(4 * a + 1, eb[1]); SB();
#pragma unroll
            for (int b = 0; b < 4; ++b) nx[b] = __builtin_elementwise_fma(__builtin_shufflevector(eb[b], eb[b], 1, 0), m32.n, t[b]);
            SB();
            PUTB(4 * a + 2, eb[2]); PUTB(4 * a + 3, eb[3]); SB();
#pragma unroll
            for (int b = 0; b < 4; ++b) eb[b] = nx[b];
        } else { PUTB(28, eb[0]); PUTB(29, eb[1]); PUTB(30, eb[2]); PUTB(31, eb[3]); }
    }
    (void)mm;
}
struct KgG { f32x2 g[21]; float S, invS; };
DI void kg_g(KgG& o, const KgMode& m, const KgIn& k, int hf) {
    const float cr = (hf ? bfhi(k.cre) : bflo(k.cre)) * m.dt, ci = (hf ? bfhi(k.cim) : bflo(k.cim)) * m.dt;
    const float bound = 2.f * m.dt * wave_sum(fabsf(bflo(k.cre)) + fabsf(bfhi(k.cre)) + fabsf(bflo(k.cim)) + fabsf(bfhi(k.cim))) + fabsf(__uint_as_float(k.dsk));
    int e = (int)(__float_as_uint(bound) >> 23) - 127;
    e = e < -100 ? -100 : (e > 100 ? 100 : e);
    o.S = __uint_as_float((unsigned)(127 + 13 - e) << 23); o.invS = __uint_as_float((unsigned)(127 - 13 + e) << 23);
    const f32x2 w1 = cexpp(m.er, m.rv, 1.f), w256 = cexpp(m.er, m.rv, 256.f);
    const CM m1 = cm(w1), m256 = cm(w256);
    f32x2* g = o.g;
    g[0] = f32x2{2.f * o.S * cr, 2.f * o.S * ci};
    f32x2 r4[4]; cmuln<4>(r4, {w1, w256, g[0], g[0]}, {m1, m256, m1, m256});
    g[1] = r4[2]; g[8] = r4[3];
    const CM m2 = cm(r4[0]), m512 = cm(r4[1]);
    f32x2 r6[6]; cmuln<6>(r6, {r4[0], g[0], g[1], g[8], g[0], g[8]}, {m2, m2, m2, m2, m512, m512});
    g[2] = r6[1]; g[3] = r6[2]; g[9] = r6[3]; g[12] = r6[4]; g[14] = r6[5];
    const CM m4 = cm(r6[0]);
    f32x2 ra[6]; cmuln<6>(ra, {g[12], g[14], g[0], g[1], g[2], g[3]}, {m512, m512, m4, m4, m4, m4});
    g[16] = ra[0]; g[17] = ra[1]; g[4] = ra[2]; g[5] = ra[3]; g[6] = ra[4]; g[7] = ra[5];
    f32x2 rb[6]; cmuln<6>(rb, {g[16], g[17], g[8], g[9], g[12], g[14]}, {m512, m512, m4, m4, m4, m4});
    g[18] = rb[0]; g[19] = rb[1]; g[10] = rb[2]; g[11] = rb[3]; g[13] = rb[4]; g[15] = rb[5];
    g[20] = cmul(g[18], m512);
}
DI void kg_put_g(const KgG& o, int lane, lds_ptr tab) {
    lds_ptr TW = tab + lane * 4;
#pragma unroll
    for (int r = 0; r < 21; ++r) PUTA(r, o.g[r]);
}
#undef PUTA
#undef PUTB
DI void kg_read(f16x8 (&f)[8], int lane, lds_ptr tab) {
#pragma unroll
    for (int ks = 0; ks < 8; ++ks) f[ks] = *(LAS f16x8*)(tab + (lane & 31) * KG_ROW + (lane >> 5) * 16 + ks * 32);
}
constexpr int KT_STRIDE = 1408;
DI void kgen_channel(const KgIn& k, int d, int lane, lds_ptr tab, lds_ptr kt, srd_t sws, syp prev_flag, unsigned tag) {
    f32x16 acc = {};
    float S, invS;
#pragma unroll 1
    for (int hf = 0; hf < 2; ++hf) {
        const KgMode m = kg_mode(k, hf);
        kg_e(m, lane, tab);
        KgG g; kg_g(g, m, k, hf);
        S = g.S; invS = g.invS;
        WAIT_LG();
        f16x8 a[8], b[8];
        kg_read(b, lane, tab);
        WAIT_LG();
        kg_put_g(g, lane, tab);
        WAIT_LG();
        kg_read(a, lane, tab);
#pragma unroll
        for (int ks = 0; ks < 8; ++ks) acc = __builtin_amdgcn_mfma_f32_32x32x16_f16(a[ks], b[ks], acc, 0, 0, 0);
        WAIT_LG();
        if (prev_flag != nullptr && hf == 0) publish(prev_flag, tag, lane);
    }
    lds_ptr TS = kt + KT_S;
    const int col = lane & 31, hh = lane >> 5;
    if (lane == 0) { acc[0] += __uint_as_float(k.dsk) * S; *(LAS float*)(kt + KT_INV) = invS; }
    *(LAS u32x2*)(kt + 8 * lane) = u32x2{pk_f16(acc[3], acc[2]), pk_f16(acc[1], acc[0])};
    if (hh == 0) {
        *(LAS u32x2*)(TS + 8 * col) = u32x2{pk_f16(acc[4], acc[5]), pk_f16(acc[6], acc[7])};
#pragma unroll
        for (int i = 0; i < 4; ++i) *(LAS _Float16*)(TS + 2 * (256 + 32 * i + col)) = (_Float16)acc[8 + i];
    } else {
        *(LAS unsigned*)(TS + 2 * (128 + 2 * col)) = pk_f16(acc[4], acc[5]);
        *(LAS unsigned*)(TS + 2 * (192 + 2 * col)) = pk_f16(acc[6], acc[7]);
        if (col == 0) *(LAS unsigned*)(TS + 2 * 384) = pk_f16(acc[8], 0.f);
    }
    WAIT_LG();
    const u32x4 o0 = *(LAS u32x4*)(kt + 16 * lane), o1 = *(LAS u32x4*)(kt + 1024 + 16 * (lane < 18 ? lane : 0));
    st16_dev(o0, sws, 16 * lane, WS_KT + d * KT_STRIDE);
    if (lane < 18) st16_dev(o1, sws, 16 * lane, WS_KT + d * KT_STRIDE + 1024);
}

constexpr int CV_COPY = 4160, CV_U = 4 * CV_COPY, CV_BYTES = CV_U + 4608;
struct ConvIn { u32x4 uu[4]; u32x2 z, a[3]; unsigned inv; };
DI void conv_load(ConvIn& c, int d, int lane, srd_t sws) {
    const unsigned U = WS_UT + (unsigned)d * 128, vu = (lane >> 3) * (D_ * 128) + (lane & 7) * 16, T = WS_KT + d * KT_STRIDE;
    c.z = ld8_dev(sws, 8 * lane, T);
#pragma unroll
    for (int sg = 0; sg < 3; ++sg) c.a[sg] = ld8_dev(sws, 4 * lane, T + KT_S + 256 * sg);
    c.inv = __builtin_amdgcn_raw_buffer_load_b32(sws, 0, T + KT_INV, SC1);
#pragma unroll
    for (int i = 0; i < 4; ++i) c.uu[i] = ld16_dev(sws, vu, U + i * (8 * D_ * 128));
}
DI void conv_stage_k(const ConvIn& c, int lane, lds_ptr lds) {
    const unsigned a0[3] = {c.a[0][0], c.a[1][0], c.a[2][0]}, a1[3] = {c.a[0][1], c.a[1][1], c.a[2][1]};
    if (lane < 30) *(LAS u32x4*)(lds + CV_U + 16 * lane) = u32x4{0, 0, 0, 0};
    else if (lane < 46) *(LAS u32x4*)(lds + ((lane - 30) >> 2) * CV_COPY + 4096 + 16 * ((lane - 30) & 3)) = u32x4{0, 0, 0, 0};
    *(LAS u32x2*)(lds + 2 * (2044 - 8 * (lane & 31) - 4 * (lane >> 5))) = c.z;
#define PK(sg, lo, hi) __builtin_amdgcn_perm(a1[sg], a0[sg], (unsigned)((2 * (lo)) | ((2 * (lo) + 1) << 8) | ((2 * (hi)) << 16) | ((2 * (hi) + 1) << 24)))
    *(LAS u32x2*)(lds + 2 * (1788 - 4 * lane)) = u32x2{PK(0, 2, 1), PK(0, 0, 0)};
    *(LAS u32x4*)(lds + 2 * (1528 - 8 * lane)) = u32x4{PK(1, 2, 2), PK(1, 1, 1), PK(1, 1, 0), PK(1, 0, 0)};
    { const unsigned t22 = PK(2, 2, 2), t11 = PK(2, 1, 1), t10 = PK(2, 1, 0), t00 = PK(2, 0, 0);
      *(LAS u32x4*)(lds + 2 * (1008 - 16 * lane)) = u32x4{t22, t22, t11, t11};
      *(LAS u32x4*)(lds + 2 * (1008 - 16 * lane + 8)) = u32x4{t11, t10, t00, t00}; }
#undef PK
    WAIT_LG();
    u32x4 e4[4]; u32x2 e2[4];
#pragma unroll
    for (int i = 0; i < 4; ++i) { e4[i] = *(LAS u32x4*)(lds + 1024 * i + 16 * lane); e2[i] = *(LAS u32x2*)(lds + 1024 * i + 16 * lane + 16); SB(); }
#pragma unroll
    for (int i = 0; i < 4; ++i) {
        const u32x4 e = e4[i]; const u32x2 f = e2[i];
        const unsigned o0 = __builtin_amdgcn_alignbit(e[1], e[0], 16), o1 = __builtin_amdgcn_alignbit(e[2], e[1], 16), o2 = __builtin_amdgcn_alignbit(e[3], e[2], 16),
                       o3 = __builtin_amdgcn_alignbit(f[0], e[3], 16), o4 = __builtin_amdgcn_alignbit(f[1], f[0], 16);
        lds_ptr q = lds + 1024 * i + 16 * lane;
        *(LAS u32x4*)(q + CV_COPY) = u32x4{o0, o1, o2, o3};
        *(LAS u32x4*)(q + 2 * CV_COPY) = u32x4{e[1], e[2], e[3], f[0]};
        *(LAS u32x4*)(q + 3 * CV_COPY) = u32x4{o1, o2, o3, o4};
    }
}
DI void conv_stage_u(const ConvIn& c, int lane, lds_ptr lds) {
#pragma unroll
    for (int i = 0; i < 4; ++i) *(LAS u32x4*)(lds + CV_U + 480 + 1024 * i + 16 * lane) = c.uu[i];
    WAIT_LG();
}
DI constexpr int cv_dl(int m) { return m < 8 ? 0 : m < 15 ? 1 : m < 21 ? 2 : m < 26 ? 3 : m < 30 ? 4 : m < 33 ? 5 : m < 35 ? 6 : 7; }
DI constexpr int cv_cb(int m) { const int st[8] = {0, 8, 15, 21, 26, 30, 33, 35}; return 7 - cv_dl(m) - (m - st[cv_dl(m)]); }
DI constexpr int cv_ra(int m) { return m < 2 ? 1 : m == 5 || m == 6 ? 2 : m == 11 || m == 12 ? 3 : m == 17 || m == 18 ? 4 : m == 19 || m == 20 ? 5 : m == 23 || m == 24 ? 6 : m == 25 || m == 26 ? 7 : m == 28 || m == 29 ? 8 : -1; }
DI constexpr int cv_rh(int m) { return m == 1 || m == 6 || m == 12 || m == 18 || m == 20 || m == 24 || m == 26 || m == 29; }
DI constexpr int cv_rb(int m) { return m == 2 ? 7 : m == 9 ? 6 : m == 16 ? 5 : m == 22 ? 4 : m == 27 ? 3 : m == 31 ? 2 : m == 34 ? 1 : m == 35 ? 0 : -1; }
struct ConvRegs { u32x2 al[8], ah[8]; f16x8 b[8]; f32x4 acc[8]; };
template <int S, int M> DI void cv_slot(ConvRegs& r, lds_ptr pa, lds_ptr pb) {
    constexpr int dl = cv_dl(M), cb = cv_cb(M), ra = cv_ra(M), rb = cv_rb(M);
    r.acc[dl + cb] = __builtin_amdgcn_mfma_f32_16x16x32_f16(__builtin_bit_cast(f16x8, u32x4{r.al[dl][0], r.al[dl][1], r.ah[dl][0], r.ah[dl][1]}), r.b[cb], r.acc[dl + cb], 0, 0, 0);
    if constexpr (ra >= 1 && ra <= 7) { if constexpr (cv_rh(M)) r.ah[ra] = *(LAS u32x2*)(pa + 2 * (256 * (7 - ra) + 32 * (7 - S)) + 8); else r.al[ra] = *(LAS u32x2*)(pa + 2 * (256 * (7 - ra) + 32 * (7 - S))); }
    if constexpr (ra == 8 && S < 7) { if constexpr (cv_rh(M)) r.ah[0] = *(LAS u32x2*)(pa + 2 * (256 * 7 + 32 * (6 - S)) + 8); else r.al[0] = *(LAS u32x2*)(pa + 2 * (256 * 7 + 32 * (6 - S))); }
    if constexpr (rb >= 0 && S < 7) r.b[rb] = *(LAS f16x8*)(pb + 2 * (256 * rb + 32 * (6 - S)));
    SB();
}
template <int S, int... M> DI void cv_step(ConvRegs& r, lds_ptr pa, lds_ptr pb, std::integer_sequence<int, M...>) { (cv_slot<S, M>(r, pa, pb), ...); }
template <int... S> DI void cv_steps(ConvRegs& r, lds_ptr pa, lds_ptr pb, std::integer_sequence<int, S...>) { (cv_step<S>(r, pa, pb, std::make_integer_sequence<int, 36>{}), ...); }
DI void conv_run(int d, float inv, int lane, lds_ptr lds, srd_t sws) {
    const int b = lane & 15, h = lane >> 4;
    lds_ptr pa = lds + (3 - (b & 3)) * CV_COPY + 2 * (2052 - 8 * h - 4 * (b >> 2)) - 2 * (256 * 7 + 32 * 7);
    lds_ptr pb = lds + CV_U + 2 * (240 + 16 * b - 8 * h + 8) - 2 * 32 * 7;
    ConvRegs r;
#pragma unroll
    for (int c = 0; c < 8; ++c) r.acc[c] = f32x4{0.f, 0.f, 0.f, 0.f};
    r.al[0] = *(LAS u32x2*)(pa + 2 * (256 * 7 + 32 * 7)); SB(); r.ah[0] = *(LAS u32x2*)(pa + 2 * (256 * 7 + 32 * 7) + 8); SB();
#pragma unroll
    for (int c = 7; c >= 0; --c) r.b[c] = *(LAS f16x8*)(pb + 2 * (256 * c + 32 * 7));
    SB();
    cv_steps(r, pa, pb, std::make_integer_sequence<int, 8>{});
    const unsigned S = WS_ST + (unsigned)d * 128, vs = (b >> 2) * (D_ * 128) + (b & 3) * 32 + 8 * h;
#pragma unroll
    for (int c = 0; c < 8; ++c) st8_dev(u32x2{pk_bf16(r.acc[c][0] * inv, r.acc[c][1] * inv), pk_bf16(r.acc[c][2] * inv, r.acc[c][3] * inv)}, sws, vs, S + c * (4 * D_ * 128));
}

template <int NP = 8> DI void wd_issue(int u, int lane, unsigned ldsaddr, srd_t swd, int i0 = 0) {
    ldsaddr = __builtin_amdgcn_readfirstlane(ldsaddr);
    const int fs = u / 5, cb = u % 5;
#pragma unroll
    for (int ii = 0; ii < NP; ++ii) { const int i = i0 + ii, row = 4 * i + (lane >> 4), p16 = lane & 15;
        dma16_nt(ldsaddr + i * 1024, (unsigned)(row * 1280 + 16 * ((((p16 >> 1) ^ (row & 7)) << 1) | (p16 & 1))), swd, (unsigned)(fs * 32 * 1280 + cb * 256)); }
}
template <int NP = 8> DI void wd_finish(int u, int lane, lds_ptr lds, srd_t sws, int i0 = 0) {
    const int fs = u / 5, cb = u % 5;
    const int kq = lane >> 4, q = (lane & 15) >> 2, pp = lane & 3, row = 4 * kq + q;
    u32x2 ta[NP], tb[NP];
#pragma unroll
    for (int i = 0; i < NP; ++i) { ta[i] = __builtin_bit_cast(u32x2, tr_read(lds + row * 256 + 32 * ((i0 + i) ^ (row & 7)) + 8 * pp));
                                   tb[i] = __builtin_bit_cast(u32x2, tr_read(lds + (16 + row) * 256 + 32 * ((i0 + i) ^ (row & 7)) + 8 * pp)); }
#pragma unroll
    for (int i = 0; i < NP; ++i) {
        const int d16 = 8 * cb + i0 + i;
        st16_dev(u32x4{ta[i][0], ta[i][1], tb[i][0], tb[i][1]}, sws, lane * 16, WS_WD + (unsigned)(((d16 / 5) * 54 + fs) * 5 + d16 % 5) * 1024);
    }
    WAIT_LG();
}

constexpr int G1_SLOT = 49152, G1_WG = 32768, G1_WU = 40960;
constexpr int AUX_W = LDS_BYTES - 128, AUX_GO = AUX_W, AUX_TAG = AUX_W + 4, AUX_KC = AUX_W + 16, AUX_DONE = AUX_W + 32, AUX_RS = AUX_W + 64;
constexpr int G2_RING = 3 * CV_BYTES, G2_HALF = 15360;
constexpr int AUX_CS = G2_RING + 6 * G2_HALF, AUX_ST = AUX_CS + 1024;
constexpr int g2_slot(int st) { return (st % 5) < 3 ? G2_RING + (st % 5) * 2 * G2_HALF : ((st % 5) - 3) * 2 * G2_HALF; }
constexpr int G2_S = g2_slot(4), G2_X = G2_S + 10240, G2_O = g2_slot(6);
constexpr int WD_TMP = 3 * G1_SLOT;
constexpr int HW_BASE = 65536, HK_BASE = 65536 + 16384;
static_assert(AUX_ST + 1024 <= AUX_W && HK_BASE + 8 * (KG_BYTES + KT_BYTES) <= AUX_W, "LDS map");
struct Frag { u32x4 x[4]; s16x4 wl[4], wh[4]; };
#define MF16(acc, lo, hi, X) acc = __builtin_amdgcn_mfma_f32_16x16x32_bf16(__builtin_bit_cast(bf16x8, s16x8{lo[0], lo[1], lo[2], lo[3], hi[0], hi[1], hi[2], hi[3]}), __builtin_bit_cast(bf16x8, X), acc, 0, 0, 0)
template <int NV> DI void wait_h(u32x4& a) { asm volatile("s_waitcnt vmcnt(%1)" : "+v"(a) : "n"(NV) : "memory"); }
template <int I> using IC = std::integral_constant<int, I>;
struct G2Regs { u32x4 hb[HD]; bf16x8 w[2][5]; f32x4 acc[5]; };
template <int T> DI void g2_step(G2Regs& r, lds_ptr pw, int lane, srd_t sws, unsigned soh) {
    constexpr int nx = T + 1;
    if constexpr (T % 3 == 2 && T != 5 && T < 26) { LBAR(); SB(); }
    wait_h<(T + HD <= 27 ? HD - 1 : 26 - T)>(r.hb[T % HD]); SB();
#pragma unroll
    for (int j = 0; j < 5; ++j) {
        r.acc[j] = __builtin_amdgcn_mfma_f32_16x16x32_bf16(r.w[T & 1][j], __builtin_bit_cast(bf16x8, r.hb[T % HD]), r.acc[j], 0, 0, 0);
        if constexpr (nx < 27) r.w[nx & 1][j] = *(LAS bf16x8*)(pw + g2_slot(nx / 3) + ((nx % 3) * 5 + j) * 1024);
        SB();
    }
    if constexpr (T + HD < 27) { hload(r.hb[T % HD], lane * 16, sws, soh + (T + HD) * 4096); SB(); }
}
template <int B, int... T> DI void g2_steps(G2Regs& r, lds_ptr pw, int lane, srd_t sws, unsigned soh, std::integer_sequence<int, T...>) { (g2_step<B + T>(r, pw, lane, sws, soh), ...); }

__global__ __launch_bounds__(768, 3) void fused_kernel(Params p) {
    extern __shared__ __attribute__((aligned(16))) char smem[];
    lds_ptr lds = (lds_ptr)smem;
    const unsigned lds0 = (unsigned)(uintptr_t)lds;
    const int tid = threadIdx.x, lane = tid & 63, w = __builtin_amdgcn_readfirstlane(tid >> 6);
    const int xg = blockIdx.x & 7, bi = blockIdx.x >> 3;
    const bool gemm_wg = bi >= 5;
    syp sy = (syp)(p.ws + WS_SYNC);
    const srd_t sws = mk_srd(p.ws, WS_END);
    STAMP_DECL; STAMP(0);
    const int rt = 4 * xg + (bi & 3), dt = bi >> 2;
    const unsigned vl16 = lane * 16;
    auto w2_src = [&](int st, int pc) { return WS_WD + (unsigned)((dt * 54 + 3 * st) * 5 + pc + (pc >= 15 ? 27 * 5 - 15 : 0)) * 1024; };
    auto w2_stage = [&](int st, int first, int step, int np) {
#pragma unroll
        for (int i = 0; i < np; ++i) { const int pc = first + step * i; dma16_dev(lds0 + g2_slot(st) + pc * 1024, vl16, sws, w2_src(st, pc)); } };
#define LOAD_E(v) asm volatile("global_load_dword %0, %1, %2 sc1" : "=v"(v) : "v"(0u), "s"(p.ws + WS_SYNC + 128 * SY_E) : "memory")
#define TAKE_TAG(v, N) do { asm volatile("s_waitcnt vmcnt(" #N ")\n\tv_readfirstlane_b32 %0, %1\n\ts_nop 4" : "=s"(tag) : "v"(v) : "memory"); tag += 1u; } while (0)
#define READ_TAG() (tag = __builtin_amdgcn_readfirstlane(lds_ld(lds + AUX_TAG)))
    unsigned tag;
    if (gemm_wg) {
        const int t = (xg & 3) * 27 + (bi - 5), panel = (xg >> 2) * 4 + (t & 3), ft = t >> 2;
        const int ch0 = 80 * xg + 3 * (bi - 5), nch = bi == 31 ? 2 : 3, gidx = (bi - 5) * 8 + xg;
        if (w >= 8) {
            const int lw = w - 8;
            const srd_t sx = mk_srd(p.x, L_ * D_ * 2), sg = mk_srd(p.wg, D_ * F_ * 2), su = mk_srd(p.wu, D_ * F_ * 2);
            const unsigned vx0 = (lane >> 3) * 1280 + 16 * ((lane & 7) ^ (lane >> 4)), vx1 = vx0 ^ 64;
            const unsigned vw0 = (lane >> 3) * 3456 + 16 * ((lane & 7) ^ (2 * ((lane >> 4) & 1))), vw1 = vw0 ^ 64;
            const unsigned sox = (unsigned)(panel * 256 + 64 * lw) * 1280, sow = (unsigned)(16 * lw) * 3456 + ft * 128;
            const int rho = ROT1 == 2 ? (3 * (ft + panel)) % 10 : ROT1 == 3 ? (3 * ft + 5 * (panel >> 2)) % 10 : ROT1 == 4 ? (5 * ((ft + panel) & 1) + ((xg & 3) * 5 + 1) / 2) % 10 : ROT1 ? ((xg & 3) * 5 + 1) / 2 + 5 * (xg >> 2) : 0;
            auto tile = [&](int k0, unsigned slot) {
                int kt = k0 + rho; kt -= kt >= 20 ? 20 : kt >= 10 ? 10 : 0;
                dma16(lds0 + slot + G1_WG + lw * 2048, vw0, sg, sow + kt * 64 * 3456);
                dma16(lds0 + slot + G1_WU + lw * 2048, vw0, su, sow + kt * 64 * 3456);
                dma16(lds0 + slot + G1_WG + lw * 2048 + 1024, vw1, sg, sow + kt * 64 * 3456 + 8 * 3456);
                dma16(lds0 + slot + G1_WU + lw * 2048 + 1024, vw1, su, sow + kt * 64 * 3456 + 8 * 3456);
#pragma unroll
                for (int i = 0; i < 8; ++i) dma16(lds0 + slot + (8 * lw + i) * 1024, (i & 1) ? vx1 : vx0, sx, sox + i * 8 * 1280 + kt * 128);
            };
            unsigned e_raw = 0;
            if (lw == 0) LOAD_E(e_raw);
            asm volatile("s_nop 4" ::: "memory");
            tile(0, 0); tile(1, G1_SLOT);
            WAIT_BAR(12);
            tile(2, 2 * G1_SLOT);
            WAIT_BAR(12);
            tile(3, 0);
            wd_issue<2>(gidx, lane, lds0 + WD_TMP, mk_srd(p.wd, D_ * F_ * 2), 2 * lw);
            if (lw == 0) {
                TAKE_TAG(e_raw, 26);
                if (lane == 0) lds_st(lds + AUX_TAG, tag);
                unsigned long long gr = ((unsigned long long)tag << 32) | lds_ld(lds + AUX_RS + 4 * (lane < 10 ? lane : 0));
                GAS unsigned long long* gp = (GAS unsigned long long*)(p.ws + WS_STATS) + (lane < 8 ? 8 * gidx + lane : 1728 + 2 * gidx + lane - 8);
                if (lane == 10) { gr = tag; gp = (GAS unsigned long long*)(sy + SY_ACK + blockIdx.x); }
                if (lane < (gidx < 160 ? 11 : 8) || lane == 10) __hip_atomic_store(gp, gr, __ATOMIC_RELAXED, __HIP_MEMORY_SCOPE_AGENT);
                WAIT_BAR(15);
            } else WAIT_BAR(14);
            tile(4, G1_SLOT);
            WAIT_BAR(12);
            READ_TAG();
            tile(5, 2 * G1_SLOT);
            wd_finish<2>(gidx, lane, lds + WD_TMP, sws, 2 * lw);
            WAIT_BAR(14);
            tile(6, 0);
            WAIT_BAR(12);
            tile(7, G1_SLOT);
            if (lw == 3) { if (lane == 0) sy_st(sy + SY_WD + gidx, tag); WAIT_BAR(13); } else WAIT_BAR(12);
            tile(8, 2 * G1_SLOT);
            WAIT_BAR(12);
            const int ch = ch0 + lw;
            const bool cv = lw < nch;
            u32x4 pk0, pk1; unsigned pf;
            { const unsigned vo = WS_STATS + panel * 2048 + (lw == 2 ? lane * 32 : 0);
              hload(pk0, vo, sws, 0u); hload(pk1, vo + 16, sws, 0u); }
            const unsigned vpf = 128u * (!cv ? SY_ALL + xg : lane >= 32 ? SY_K + ch : SY_LN + 32 * (ch >> 7) + lane);
            asm volatile("buffer_load_dword %0, %1, %2, %3 offen sc1" : "=v"(pf) : "v"(vpf), "s"(sws), "s"(WS_SYNC) : "memory");
            tile(9, 0);
            asm volatile("s_waitcnt vmcnt(12) lgkmcnt(0)\n\ts_barrier" : "+v"(pk0), "+v"(pk1), "+v"(pf) :: "memory");
            if (lw == 3) {
                if (!__all(pf == tag)) asm volatile("buffer_load_dword %0, %1, %2, %3 offen sc1" : "=v"(pf) : "v"(vpf), "s"(sws), "s"(WS_SYNC) : "memory");
                asm volatile("s_waitcnt vmcnt(0)" : "+v"(pf) :: "memory");
                const bool wd_ready = __all(pf == tag);
                LBAR();
                if (wd_ready) w2_stage(0, 0, 1, 30);
                LBAR();
                STAMP(1);
                if (!wd_ready) { poll_all<1, 4>(sy + SY_ALL + xg, 1, tag, lane); w2_stage(0, 0, 1, 30); }
                if (WMODE == 0) lds_wait(lds + AUX_DONE, 8, 1, lane);
                if (WMODE == 3) { if (lane == 0) lds_st(lds + AUX_KC + 12, 1u); lds_wait(lds + AUX_KC, 4, 1, lane); }
                w2_stage(1, 0, 1, 30);
                WAIT_VM(30);
                if (WMODE == 1) lds_wait(lds + AUX_DONE, 8, 1, lane);
                if (lane == 0) lds_st(lds + AUX_GO + 8, 1u);
                w2_stage(2, 0, 1, 30);
                STAMP(2);
            } else {
                bool ready = cv && !NOCONV && __all(pf == tag), loaded = false;
                ConvIn c;
                if (!ready) asm volatile("buffer_load_dword %0, %1, %2, %3 offen sc1" : "=v"(pf) : "v"(vpf), "s"(sws), "s"(WS_SYNC) : "memory");
                if (lw == 2) { unsigned g[4] = {pk0[0], pk0[2], pk1[0], pk1[2]};
                    if (!__all(pk0[1] == tag && pk0[3] == tag && pk1[1] == tag && pk1[3] == tag)) stats_poll<4>(g, sws, lane * 32, WS_STATS + panel * 2048, tag);
                    *(LAS u32x4*)(lds + AUX_ST + lane * 16) = u32x4{g[0], g[1], g[2], g[3]}; }
                asm volatile("s_waitcnt vmcnt(0) lgkmcnt(0)\n\ts_barrier" : "+v"(pf) :: "memory");
                ready = cv && !NOCONV && __all(pf == tag);
                if (ready) { conv_load(c, ch, lane, sws); loaded = true; }
                LBAR();
                STAMP(1);
                if (WMODE == 3 && (!cv || NOCONV) && lane == 0) lds_st(lds + AUX_KC + 4 * lw, 1u);
                if (cv && !NOCONV) {
                    lds_ptr cs = lds + lw * CV_BYTES;
                    if (!loaded) {
                        if (WMODE == 3 && lane == 0) lds_st(lds + AUX_KC + 4 * lw, 1u);
#pragma unroll 1
                        for (int it = 0; it < (1 << 18); ++it) { if (__all(sy_ld(sy + (vpf >> 7)) == tag)) break; __builtin_amdgcn_s_sleep(SL_K); }
                        asm volatile("" ::: "memory");
                        conv_load(c, ch, lane, sws); }
                    STAMP(2);
                    __builtin_amdgcn_s_setprio(2);
                    conv_stage_k(c, lane, cs);
                    conv_stage_u(c, lane, cs);
                    if (WMODE == 3 && lane == 0) lds_st(lds + AUX_KC + 4 * lw, 1u);
                    __builtin_amdgcn_s_setprio(0);
                    STAMP(3);
                    conv_run(ch, __uint_as_float(c.inv), lane, cs, sws);
                    STAMP(4);
                }
                if (cv) publish(sy + SY_S + ch, tag, lane);
                STAMP(5);
            }
        } else {
            if (tid < 16 && tid != 1) *(LAS unsigned*)(lds + AUX_W + 4 * tid) = 0u;
            { RowIn a, b; const bool more = w < 2 && gidx < 160;
              row_load(a, p.x, 8 * gidx + w, lane);
              if (more) row_load(b, p.x, 1728 + 2 * gidx + w, lane);
              const unsigned rstat = row_stats(a); if (lane == 0) lds_st(lds + AUX_RS + 4 * w, rstat);
              if (more) { const unsigned r2 = row_stats(b); if (lane == 0) lds_st(lds + AUX_RS + 32 + 4 * w, r2); } }
            const int tg = w >> 1, fg = w & 1, kq = lane >> 4, l16 = lane & 15;
            unsigned ax[2], aw[2];
            { const int row = 64 * tg + l16, q = l16 >> 2, pp = lane & 3;
#pragma unroll
              for (int i = 0; i < 2; ++i) { const int ks = i ^ (tg >> 1);
                  ax[i] = row * 128 + 16 * ((4 * ks + kq) ^ ((row >> 1) & 7));
                  aw[i] = G1_WG + (32 * ks + 8 * kq + q) * 128 + 32 * ((2 * fg + (tg & 1)) ^ ((q >> 1) | ((kq & 1) << 1))) + 8 * pp; } }
            float csg = 0.f, csu = 0.f;
            f32x4 ag[4][2] = {}, au[4][2] = {};
            Frag A, B;
#define RX(N, sl, ks, i) N.x[i] = *(LAS u32x4*)(lds + (sl) + ax[ks] + (i) * 2048)
#define RWL(N, sl, ks, j) N.wl[j] = tr_read(lds + (sl) + (aw[ks] ^ (((j) & 1) * 32)) + ((j) >> 1) * 8192)
#define RWH(N, sl, ks, j) N.wh[j] = tr_read(lds + (sl) + (aw[ks] ^ (((j) & 1) * 32)) + ((j) >> 1) * 8192 + 512)
#define MG(C, i, j) MF16(ag[i][j], C.wl[j], C.wh[j], C.x[i])
#define MU(C, i, j) MF16(au[i][j], C.wl[2 + j], C.wh[2 + j], C.x[i])
#define G_(M, RD, R) do { M; if (RD) { R; } SB(); } while (0)
#define CSUM(acc, v, k) do { const unsigned d_ = __builtin_bit_cast(u32x2, v)[k]; acc += bflo(d_); acc2 += bfhi(d_); asm volatile("" : "+v"(acc), "+v"(acc2)); } while (0)
#define STEPC(C, N, RD, sl, ks) do { \
        { float acc2 = 0.f; \
        G_(MG(C, 0, 0), RD, RX(N, sl, ks, 0));  G_(MU(C, 0, 0); CSUM(csg, C.wl[0], 0), RD, RWL(N, sl, ks, 0)); G_(MG(C, 0, 1), RD, RWH(N, sl, ks, 0)); G_(MU(C, 0, 1); CSUM(csg, C.wl[0], 1), RD, RWL(N, sl, ks, 2)); \
        G_(MG(C, 1, 0), RD, RWH(N, sl, ks, 2)); G_(MU(C, 1, 0); CSUM(csg, C.wh[0], 0), RD, RWL(N, sl, ks, 1)); G_(MG(C, 1, 1), RD, RWH(N, sl, ks, 1)); G_(MU(C, 1, 1); CSUM(csg, C.wh[0], 1), RD, RWL(N, sl, ks, 3)); \
        csg += acc2; } { float acc2 = 0.f; \
        G_(MG(C, 2, 0), RD, RWH(N, sl, ks, 3)); G_(MU(C, 2, 0); CSUM(csu, C.wl[2], 0), RD, RX(N, sl, ks, 1));  G_(MG(C, 2, 1), RD, RX(N, sl, ks, 2));  G_(MU(C, 2, 1); CSUM(csu, C.wl[2], 1), RD, RX(N, sl, ks, 3)); \
        G_(MG(C, 3, 0), 0, (void)0); G_(MU(C, 3, 0); CSUM(csu, C.wh[2], 0), 0, (void)0); G_(MG(C, 3, 1), 0, (void)0); G_(MU(C, 3, 1); CSUM(csu, C.wh[2], 1), 0, (void)0); \
        csu += acc2; } } while (0)
#define STEP(C, N, RD, sl, ks) do { \
        G_(MG(C, 0, 0), RD, RX(N, sl, ks, 0));  G_(MU(C, 0, 0), RD, RWL(N, sl, ks, 0)); G_(MG(C, 0, 1), RD, RWH(N, sl, ks, 0)); G_(MU(C, 0, 1), RD, RWL(N, sl, ks, 2)); \
        G_(MG(C, 1, 0), RD, RWH(N, sl, ks, 2)); G_(MU(C, 1, 0), RD, RWL(N, sl, ks, 1)); G_(MG(C, 1, 1), RD, RWH(N, sl, ks, 1)); G_(MU(C, 1, 1), RD, RWL(N, sl, ks, 3)); \
        G_(MG(C, 2, 0), RD, RWH(N, sl, ks, 3)); G_(MU(C, 2, 0), RD, RX(N, sl, ks, 1));  G_(MG(C, 2, 1), RD, RX(N, sl, ks, 2));  G_(MU(C, 2, 1), RD, RX(N, sl, ks, 3)); \
        G_(MG(C, 3, 0), 0, (void)0); G_(MU(C, 3, 0), 0, (void)0); G_(MG(C, 3, 1), 0, (void)0); G_(MU(C, 3, 1), 0, (void)0); } while (0)
            LBAR();
            STAMP(1);
            RX(A, 0, 0, 0); RWL(A, 0, 0, 0); RWH(A, 0, 0, 0); RWL(A, 0, 0, 2); RWH(A, 0, 0, 2); RWL(A, 0, 0, 1); RWH(A, 0, 0, 1); RWL(A, 0, 0, 3); RWH(A, 0, 0, 3);
            RX(A, 0, 0, 1); RX(A, 0, 0, 2); RX(A, 0, 0, 3);
#pragma unroll
            for (int kt = 0; kt < 9; ++kt) {
                if (NOCS) STEP(A, B, 1, (kt % 3) * G1_SLOT, 1); else STEPC(A, B, 1, (kt % 3) * G1_SLOT, 1);
                LBAR(); SB();
                STEP(B, A, 1, ((kt + 1) % 3) * G1_SLOT, 0);
            }
            STEPC(A, B, 1, 0, 1);
            { auto a = __builtin_amdgcn_permlane16_swap(__float_as_uint(csg), __float_as_uint(csu), false, false);
              const float v = __uint_as_float(a[0]) + __uint_as_float(a[1]);
              auto b = __builtin_amdgcn_permlane32_swap(__float_as_uint(v), __float_as_uint(v), false, false);
              if (lane < 32) *(LAS float*)(lds + AUX_CS + (tg >> 1) * 512 + (lane >> 4) * 256 + (32 * fg + 16 * (tg & 1) + l16) * 4) = __uint_as_float(b[0]) + __uint_as_float(b[1]); }
            SB();
            STEP(B, A, 0, 0, 0);
            STAMP(2);
            LBAR();
            __builtin_amdgcn_s_setprio(1);
            READ_TAG();
            f32x4 cgv[2], cuv[2];
#pragma unroll
            for (int j = 0; j < 2; ++j) { lds_ptr q = lds + AUX_CS + (32 * fg + 16 * (j ^ (tg & 1)) + 4 * kq) * 4;
                cgv[j] = *(LAS f32x4*)q + *(LAS f32x4*)(q + 512); cuv[j] = *(LAS f32x4*)(q + 256) + *(LAS f32x4*)(q + 768); }
            unsigned stt[4];
#pragma unroll
            for (int i = 0; i < 4; ++i) stt[i] = *(LAS unsigned*)(lds + AUX_ST + (64 * tg + 16 * i + l16) * 4);
            const float K0 = -2.f * 0.7978845608028654f * 1.44269504088896341f, K1 = K0 * 0.044715f;
#pragma unroll
            for (int i = 0; i < 4; ++i) {
                const float ir = bfhi(stt[i]), r = __builtin_amdgcn_rcpf(ir);
                const f32x2 nm = {-bflo(stt[i]), -bflo(stt[i])}, c0 = {K0 * r, K0 * r}, c1 = {K1 * r * r * r, K1 * r * r * r}, ir2 = {ir * ir, ir * ir};
                u32x4 o;
#pragma unroll
                for (int e = 0; e < 4; ++e) {
                    const int j = e >> 1, b = 2 * (e & 1);
                    const f32x2 g1 = nm * f32x2{cgv[j][b], cgv[j][b + 1]} + f32x2{ag[i][j][b], ag[i][j][b + 1]};
                    const f32x2 u1 = nm * f32x2{cuv[j][b], cuv[j][b + 1]} + f32x2{au[i][j][b], au[i][j][b + 1]};
                    const f32x2 z = g1 * (g1 * g1 * c1 + c0);
                    const f32x2 ex = {__builtin_amdgcn_exp2f(z[0]), __builtin_amdgcn_exp2f(z[1])};
                    const f32x2 dd = ex * ir2 + ir2;
                    const f32x2 hv = g1 * u1 * f32x2{__builtin_amdgcn_rcpf(dd[0]), __builtin_amdgcn_rcpf(dd[1])};
                    o[e] = pk_bf16(hv[0], hv[1]);
                }
                if (tg & 1) o = u32x4{o[2], o[3], o[0], o[1]};
                st16_dev(o, sws, vl16, WS_H + (unsigned)(((panel * 4 + tg) * 54 + ft * 2 + fg) * 4 + i) * 1024);
            }
            if (WMODE < 2 && lane == 0) lds_st(lds + AUX_DONE + 4 * w, 1u);
            STAMP(3);
            publish(sy + SY_H + 64 * (4 * panel + tg) + 2 * ft + fg, tag, lane);
            STAMP(4);
        }
    } else {
        const int hi = bi * 8 + xg;
        unsigned e_raw;
        if (w < 8) {
            const int d0 = 8 * hi + w, d1 = d0 + 320;
            KgIn k0, k1;
            LOAD_E(e_raw);
            kgen_load(k0, p, d0, lane);
            kgen_load(k1, p, d1, lane);
            if (tid < 16) *(LAS unsigned*)(lds + AUX_W + 4 * tid) = 0u;
            TAKE_TAG(e_raw, 0);
            STAMP(1);
#pragma unroll 1
            for (int i = 0; i < NKG; ++i) {
                kgen_channel(k0, i ? d1 : d0, lane, lds + HK_BASE + w * KG_BYTES, lds + HK_BASE + 8 * KG_BYTES + w * KT_BYTES, sws, i ? sy + SY_K + d0 : nullptr, tag);
                if (i == 0) STAMP(2);
                k0 = k1;
            }
            STAMP(3);
            publish(sy + SY_K + d1, tag, lane);
            if (NKG < 2 && lane == 0) sy_st(sy + SY_K + d0, tag);
            STAMP(4);
        } else {
            const int aw = hi * 4 + (w - 8), off = (w - 8) * 16384, wb = 216 + 2 * hi + (w - 8);
            const bool wd = w < 10 && wb < 270;
            const unsigned long long t_start = __builtin_amdgcn_s_memtime();
            LOAD_E(e_raw);
            __builtin_amdgcn_s_sleep(HSLEEP);
            __builtin_amdgcn_s_setprio(HPRIO);
            asm volatile("s_nop 4" ::: "memory");
            lnt_issue(aw, lane, lds0 + off, mk_srd(p.x, L_ * D_ * 2));
            if (wd) { wd_issue(wb, lane, lds0 + HW_BASE + (w - 8) * 8192, mk_srd(p.wd, D_ * F_ * 2)); WAIT_VM(0); wd_finish(wb, lane, lds + HW_BASE + (w - 8) * 8192, sws); }
            TAKE_TAG(e_raw, 0);
            if (wd && lane == 0) sy_st(sy + SY_WD + wb, tag);
            STAMP(2);
            while (__builtin_amdgcn_s_memtime() - t_start < STDELAY) __builtin_amdgcn_s_sleep(4);
            unsigned g[8];
            stats_poll<8>(g, sws, (2 * (lane >> 4) + ((lane >> 3) & 1)) * 64, WS_STATS + (aw & 31) * 512, tag);
            WAIT_VM(0);
            STAMP(3);
            lnt_finish(aw, g, lane, lds + off, sws);
            STAMP(6);
            publish(sy + SY_LN + aw, tag, lane);
            STAMP(4);
            if (w == 10 && bi == 4) { poll_all<5, 8>(sy + SY_WD, 270, tag, lane); if (lane == 0) sy_st(sy + SY_ALL + xg, tag); }
            if (w == 11) poll_all<1, 16>(sy + SY_ALL + xg, 1, tag, lane);
            __builtin_amdgcn_s_setprio(0);
        }
        LBAR();
        if (w == 9 && lane == 0) sy_st(sy + SY_ACK + blockIdx.x, tag);
        if (w == 10 && hi == 39) { poll_all<4, 64>(sy + SY_ACK, 256, tag, lane); if (lane == 0) sy_st(sy + SY_E, tag); }
        if (w == 11) { w2_stage(0, 0, 1, 30); w2_stage(1, 0, 1, 30); WAIT_VM(30); if (lane == 0) lds_st(lds + AUX_GO + 8, 1u); w2_stage(2, 0, 1, 30); }
    }
    if (w >= 8) {
        const int lw = w - 8;
        WAIT_BAR(0);
        STAMP(6);
        if (lw > 0) {
            w2_stage(3, lw - 1, 3, 10); w2_stage(4, lw - 1, 3, 10);
            WAIT_BAR(10); w2_stage(5, lw - 1, 3, 10);
            WAIT_BAR(10); w2_stage(6, lw - 1, 3, 10);
            WAIT_BAR(10); w2_stage(7, lw - 1, 3, 10);
            WAIT_BAR(10); w2_stage(8, lw - 1, 3, 10);
            WAIT_BAR(10); WAIT_BAR(0);
        } else {
            const srd_t sx = mk_srd(p.x, L_ * D_ * 2);
            unsigned f0, f1;
            LBAR(); LBAR();
            asm volatile("buffer_load_dword %0, %1, %2, %3 offen sc1" : "=v"(f0) : "v"(128u * lane), "s"(sws), "s"(WS_SYNC + 128u * (SY_S + 80 * dt)) : "memory");
            asm volatile("buffer_load_dword %0, %1, %2, %3 offen sc1" : "=v"(f1) : "v"(128u * (64 + (lane & 15))), "s"(sws), "s"(WS_SYNC + 128u * (SY_S + 80 * dt)) : "memory");
            LBAR();
#pragma unroll
            for (int i = 0; i < 10; ++i) { const unsigned o = i * 64 + lane; dma16(lds0 + G2_X + i * 1024, (o / 10) * 1280 + (o % 10) * 16, sx, (unsigned)(rt * 64 * 1280 + dt * 160)); }
            asm volatile("s_waitcnt vmcnt(10)" : "+v"(f0), "+v"(f1) :: "memory");
            const bool s_ready = __all(f0 == tag && f1 == tag);
            auto s_tile = [&]() {
#pragma unroll
                for (int i = 0; i < 10; ++i) dma16_dev(lds0 + G2_S + i * 1024, vl16, sws, WS_ST + (unsigned)(rt * D_ + 80 * dt) * 128 + i * 1024); };
            if (s_ready) s_tile();
            LBAR(); LBAR(); LBAR();
            if (!s_ready) { poll_all<2, 2>(sy + SY_S + 80 * dt, 80, tag, lane); s_tile(); }
            STAMP(7);
            WAIT_VM(0);
        }
        LBAR();
        STAMP(8);
    } else {
        const int tt = w & 3, kh = w >> 2, q = lane >> 4;
        const unsigned soh = WS_H + (unsigned)((rt * 54 + 27 * kh) * 4 + tt) * 1024;
        __builtin_amdgcn_s_setprio(3);
        if (w == 0) { if (gemm_wg) poll_all<1, SL_H>(sy + SY_H + 64 * rt, 54, tag, lane); else poll_all<1, SL_HH>(sy + SY_H + 64 * rt, 54, tag, lane);
                      if (lane == 0) lds_st(lds + AUX_GO, 1u); }
        else lds_wait(lds + AUX_GO, 1, 1, lane);
        STAMP(5);
        G2Regs r;
        lds_ptr pw = lds + kh * G2_HALF + vl16;
#pragma unroll
        for (int k = 0; k < HD; ++k) { hload(r.hb[k], vl16, sws, soh + k * 4096); if (k == 1) __builtin_amdgcn_s_setprio(1); }
        lds_wait(lds + AUX_GO + 8, 1, 1, lane);
#pragma unroll
        for (int j = 0; j < 5; ++j) { r.acc[j] = f32x4{0.f, 0.f, 0.f, 0.f}; r.w[0][j] = *(LAS bf16x8*)(pw + g2_slot(0) + j * 1024); }
        SB();
        g2_steps<0>(r, pw, lane, sws, soh, std::make_integer_sequence<int, 2>{});
        G2STAMP(6);
        g2_steps<2>(r, pw, lane, sws, soh, std::make_integer_sequence<int, 1>{});
        G2STAMP(7);
        g2_steps<3>(r, pw, lane, sws, soh, std::make_integer_sequence<int, 6>{});
        G2STAMP(11);
        g2_steps<9>(r, pw, lane, sws, soh, std::make_integer_sequence<int, 9>{});
        G2STAMP(13);
        g2_steps<18>(r, pw, lane, sws, soh, std::make_integer_sequence<int, 9>{});
        STAMP(8);
        lds_ptr px = lds + g2_slot(5) + tt * 5120 + vl16;
        if (kh) { *(LAS f32x4*)(px) = r.acc[0]; *(LAS f32x4*)(px + 1024) = r.acc[1]; *(LAS f32x4*)(px + 2048) = r.acc[2]; }
        else    { *(LAS f32x4*)(px + 3072) = r.acc[3]; *(LAS f32x4*)(px + 4096) = r.acc[4]; }
        LBAR();
        STAMP(9);
        auto fin = [&](int j, f32x4 a) {
            const int ro = (tt * 16 + (lane & 15)) * 160 + 32 * j + 8 * q;
            const u32x2 xx = *(LAS u32x2*)(lds + G2_X + ro);
            a += *(LAS f32x4*)(px + j * 1024);
            const s16x4 sv = tr_read(lds + G2_S + (16 * j + 4 * q + ((lane & 15) >> 2)) * 128 + tt * 32 + (lane & 3) * 8);
            float o[4];
#pragma unroll
            for (int i = 0; i < 4; ++i) {
                const float sg = a[i] * __builtin_amdgcn_rcpf(1.f + __builtin_amdgcn_exp2f(-1.44269504088896341f * a[i]));
                o[i] = fmaf(bf2f((unsigned short)sv[i]), sg, (i & 1) ? bfhi(xx[i >> 1]) : bflo(xx[i >> 1]));
            }
            *(LAS u32x2*)(lds + G2_O + ro) = u32x2{pk_bf16(o[0], o[1]), pk_bf16(o[2], o[3])};
        };
        if (kh == 0) { fin(0, r.acc[0]); fin(1, r.acc[1]); fin(2, r.acc[2]); } else { fin(3, r.acc[3]); fin(4, r.acc[4]); }
    }
    LBAR();
    if (w < 10) { const u32x4 ov = *(LAS u32x4*)(lds + G2_O + tid * 16);
        asm volatile("global_store_dwordx4 %0, %1, off sc0 sc1\n\ts_nop 1" :: "v"(p.out + (size_t)(rt * 64 + tid / 10) * D_ + 80 * dt + (tid % 10) * 8), "v"(ov) : "memory"); }
    STAMP(10);
    STAMP_FLUSH(p.ws, (gemm_wg ? STWG : STWH));
}

extern "C" void kernel_launch(void* const* d_in, const int* in_sizes, int n_in, void* d_out, int out_size, void* d_ws, size_t ws_size,
                              hipStream_t stream) {
    Params p;
    p.x = (cu16p)d_in[0]; p.lar = (cu16p)d_in[1]; p.aim = (cu16p)d_in[2]; p.cre = (cu16p)d_in[3]; p.cim = (cu16p)d_in[4];
    p.ldt = (cu16p)d_in[5]; p.dsk = (cu16p)d_in[6]; p.wg = (cu16p)d_in[8]; p.wu = (cu16p)d_in[9]; p.wd = (cu16p)d_in[10];
    p.out = (unsigned short*)d_out; p.ws = (char*)d_ws;
    (void)hipFuncSetAttribute((const void*)fused_kernel, hipFuncAttributeMaxDynamicSharedMemorySize, LDS_BYTES);
    fused_kernel<<<256, 768, LDS_BYTES, stream>>>(p);
}
```

```cpp
#include <hip/hip_runtime.h>
#include <hip/hip_bf16.h>
#include <stdint.h>
#include <type_traits>
#include <utility>

#define DI __device__ __forceinline__
#define LAS __attribute__((address_space(3)))
#define GAS __attribute__((address_space(1)))
typedef short s16x8 __attribute__((ext_vector_type(8)));
typedef short s16x4 __attribute__((ext_vector_type(4)));
typedef __bf16 bf16x8 __attribute__((ext_vector_type(8)));
typedef __bf16 bf16x2 __attribute__((ext_vector_type(2)));
typedef _Float16 f16x8 __attribute__((ext_vector_type(8)));
typedef _Float16 f16x2 __attribute__((ext_vector_type(2)));
typedef float f32x16 __attribute__((ext_vector_type(16)));
typedef float f32x4 __attribute__((ext_vector_type(4)));
typedef float f32x2 __attribute__((ext_vector_type(2)));
typedef unsigned u32x4 __attribute__((ext_vector_type(4)));
typedef unsigned u32x2 __attribute__((ext_vector_type(2)));
typedef LAS char* lds_ptr;
typedef const unsigned short* cu16p;
typedef __amdgpu_buffer_rsrc_t srd_t;

constexpr int L_ = 2048, D_ = 640, N_ = 128, F_ = 1728;
constexpr unsigned WS_SYNC = 0;
constexpr unsigned WS_STATS = 512u << 10;
constexpr unsigned WS_CS = 1536u << 10;
constexpr unsigned WS_KT = 576u << 10;
constexpr unsigned WS_H = 2u << 20;
constexpr unsigned WS_UT = 9u << 20;
constexpr unsigned WS_ST = 12u << 20;
constexpr unsigned WS_WD = 15u << 20;
constexpr unsigned WS_END = 18u << 20;
constexpr size_t WS_STAMP = (size_t)32 << 20;
constexpr int SY_E = 0, SY_ACK = 1, SY_LN = 257, SY_WD = 417, SY_ALL = 687, SY_S = 695, SY_K = 1335, SY_H = 1975;
static_assert((SY_H + 2048) * 128 <= WS_STATS, "sync map");
constexpr int SC1 = 16;
constexpr int LDS_BYTES = 163840;

#define STAMP_DECL do {} while (0)
#define STAMP(i) do {} while (0)
#define STAMP_FLUSH(ws, wv) do {} while (0)
#define TOUCH 1
#define NOCS 0
#define G2STAMP(i) STAMP(i)
#define NOHELP 0
#define HSLEEP 6
#define SL_ST 4
#define SL_K 3
#define SL_S 16
#define SL_H 1
#define SL_HH 16
#define ROT1 0
#define ROT2 0
#define FULLP 22
#define WNT 1
#define WMODE 4
#define HD 4
#define WLAG 4
#define GSLEEP 0
#define PNT 1
#define WSLEEP 0
#define WF1 10
#define T1HOLD 0
#define W3EARLY 1
#define HPRIO 2
#define STDELAY 5000
#define KPRIO 1
#define PRE1 9
#define HNT 0
#define CVWAIT 2
#define STPRIO 2
#define NGK 2
#define G2S 1
#define TOUCH1 1
#define TLANES 26
#define NKG 2
#define NOCONV 0
#define WLATE 0
#define SB() __builtin_amdgcn_sched_barrier(0)

DI float bf2f(unsigned short v) { return __uint_as_float((unsigned)v << 16); }
DI float bflo(unsigned v) { return __uint_as_float(v << 16); }
DI float bfhi(unsigned v) { return __uint_as_float(v & 0xffff0000u); }
DI float rbf(float x) { return (float)(__bf16)x; }
DI unsigned pk_bf16(float lo, float hi) { bf16x2 v = {(__bf16)lo, (__bf16)hi}; return __builtin_bit_cast(unsigned, v); }
DI unsigned pk_f16(float lo, float hi) { f16x2 v = {(_Float16)lo, (_Float16)hi}; return __builtin_bit_cast(unsigned, v); }
DI s16x4 tr_read(lds_ptr p) { return __builtin_amdgcn_ds_read_tr16_b64_v4i16((LAS s16x4*)p); }
DI srd_t mk_srd(const void* p, unsigned bytes) { return __builtin_amdgcn_make_buffer_rsrc((void*)p, (short)0, (int)bytes, 0x00020000); }
DI void dma16(unsigned ldsaddr, unsigned voff, srd_t srd, unsigned soff) {
    asm volatile("s_mov_b32 m0, %0\n\ts_nop 0\n\tbuffer_load_dwordx4 %1, %2, %3 offen lds" :: "s"(ldsaddr), "v"(voff), "s"(srd), "s"(soff) : "memory");
}
DI void dma16_nt(unsigned ldsaddr, unsigned voff, srd_t srd, unsigned soff) {
    if (WNT) asm volatile("s_mov_b32 m0, %0\n\ts_nop 0\n\tbuffer_load_dwordx4 %1, %2, %3 offen nt lds" :: "s"(ldsaddr), "v"(voff), "s"(srd), "s"(soff) : "memory");
    else dma16(ldsaddr, voff, srd, soff);
}
DI void dma16_dev(unsigned ldsaddr, unsigned voff, srd_t srd, unsigned soff) {
    asm volatile("s_mov_b32 m0, %0\n\ts_nop 0\n\tbuffer_load_dwordx4 %1, %2, %3 offen sc1 lds" :: "s"(ldsaddr), "v"(voff), "s"(srd), "s"(soff) : "memory");
}
DI void hload(u32x4& dst, unsigned vo, srd_t srd, unsigned so) { asm volatile("buffer_load_dwordx4 %0, %1, %2, %3 offen sc1" : "=v"(dst) : "v"(vo), "s"(srd), "s"(so) : "memory"); }
DI u32x4 ld16_dev(srd_t s, unsigned vo, unsigned so) { return __builtin_amdgcn_raw_buffer_load_b128(s, vo, so, SC1); }
DI u32x2 ld8_dev(srd_t s, unsigned vo, unsigned so) { return __builtin_amdgcn_raw_buffer_load_b64(s, vo, so, SC1); }
DI void st16_dev(u32x4 v, srd_t s, unsigned vo, unsigned so) {
    asm volatile("buffer_store_dwordx4 %0, %1, %2, %3 offen sc1\n\ts_nop 1" :: "v"(v), "v"(vo), "s"(s), "s"(so)); }
DI void st16_h(u32x4 v, srd_t s, unsigned vo, unsigned so) {
    if (HNT) asm volatile("buffer_store_dwordx4 %0, %1, %2, %3 offen sc1 nt\n\ts_nop 1" :: "v"(v), "v"(vo), "s"(s), "s"(so)); else st16_dev(v, s, vo, so); }
DI void st8_dev(u32x2 v, srd_t s, unsigned vo, unsigned so) { __builtin_amdgcn_raw_buffer_store_b64(v, s, vo, so, SC1); }
#define WAIT_BAR(N) asm volatile("s_waitcnt vmcnt(" #N ") lgkmcnt(0)\n\ts_barrier" ::: "memory")
#define LBAR() asm volatile("s_waitcnt lgkmcnt(0)\n\ts_barrier" ::: "memory")
#define WAIT_VM(N) asm volatile("s_waitcnt vmcnt(" #N ")" ::: "memory")
#define WAIT_LG() asm volatile("s_waitcnt lgkmcnt(0)" ::: "memory")
struct alignas(128) SyncLine { unsigned v; };
typedef GAS SyncLine* syp;
DI unsigned sy_ld(syp p) { return __hip_atomic_load(&p->v, __ATOMIC_RELAXED, __HIP_MEMORY_SCOPE_AGENT); }
DI void sy_st(syp p, unsigned v) { __hip_atomic_store(&p->v, v, __ATOMIC_RELAXED, __HIP_MEMORY_SCOPE_AGENT); }
template <int K, int SLP = 2> DI void poll_all(syp p, int n, unsigned tag, int lane) {
#pragma unroll 1
    for (int it = 0; it < (1 << 18); ++it) {
        bool ok = true;
#pragma unroll
        for (int k = 0; k < K; ++k) { const int i = lane + 64 * k; ok &= sy_ld(p + (i < n ? i : n - 1)) == tag; }
        if (__all(ok)) break;
        __builtin_amdgcn_s_sleep(SLP);
    }
    asm volatile("" ::: "memory");
}
DI unsigned lds_ld(lds_ptr p) { return __hip_atomic_load((LAS unsigned*)p, __ATOMIC_RELAXED, __HIP_MEMORY_SCOPE_WORKGROUP); }
DI void lds_st(lds_ptr p, unsigned v) { __hip_atomic_store((LAS unsigned*)p, v, __ATOMIC_RELAXED, __HIP_MEMORY_SCOPE_WORKGROUP); }
DI void lds_wait(lds_ptr p, int n, unsigned v, int lane) {
#pragma unroll 1
    for (int it = 0; it < (1 << 22); ++it) {
        if (__all(lds_ld(p + 4 * (lane & (n - 1))) >= v)) break;
        __builtin_amdgcn_s_sleep(1);
    }
    asm volatile("" ::: "memory");
}
DI void publish(syp p, unsigned tag, int lane) { WAIT_VM(0); if (lane == 0) sy_st(p, tag); }
DI float dot2bf(unsigned a, unsigned b, float c) { return __builtin_amdgcn_fdot2_f32_bf16(__builtin_bit_cast(bf16x2, a), __builtin_bit_cast(bf16x2, b), c, false); }

template <int CTRL, int RM> DI float dpp_add(float v) { return v + __builtin_bit_cast(float, __builtin_amdgcn_update_dpp(0, __builtin_bit_cast(int, v), CTRL, RM, 0xf, true)); }
DI float wave_sum(float v) {
    v = dpp_add<0x111, 0xf>(v); v = dpp_add<0x112, 0xf>(v); v = dpp_add<0x114, 0xf>(v); v = dpp_add<0x118, 0xf>(v);
    v = dpp_add<0x142, 0xa>(v); v = dpp_add<0x143, 0xc>(v);
    return __builtin_bit_cast(float, __builtin_amdgcn_readlane(__builtin_bit_cast(int, v), 63));
}
DI float sum_8_16_32(float v) {
    v = dpp_add<0x128, 0xf>(v);
    auto a = __builtin_amdgcn_permlane16_swap(__float_as_uint(v), __float_as_uint(v), false, false); v = __uint_as_float(a[0]) + __uint_as_float(a[1]);
    auto b = __builtin_amdgcn_permlane32_swap(__float_as_uint(v), __float_as_uint(v), false, false); return __uint_as_float(b[0]) + __uint_as_float(b[1]);
}

struct Params { cu16p x, lar, aim, cre, cim, ldt, dsk, wg, wu, wd; unsigned short* out; char* ws; };

DI unsigned row_stats4(cu16p x, int row, int lane) {
    const u32x4* src = (const u32x4*)(x + (size_t)row * D_) + (lane & 15);
    u32x4 v[5];
#pragma unroll
    for (int i = 0; i < 5; ++i) v[i] = src[16 * i];
    float s0 = 0.f, s1 = 0.f, q0 = 0.f, q1 = 0.f;
#pragma unroll
    for (int i = 0; i < 5; ++i) {
        s0 = dot2bf(v[i][0], 0x3f803f80u, s0); q0 = dot2bf(v[i][0], v[i][0], q0); s1 = dot2bf(v[i][1], 0x3f803f80u, s1); q1 = dot2bf(v[i][1], v[i][1], q1);
        s0 = dot2bf(v[i][2], 0x3f803f80u, s0); q0 = dot2bf(v[i][2], v[i][2], q0); s1 = dot2bf(v[i][3], 0x3f803f80u, s1); q1 = dot2bf(v[i][3], v[i][3], q1);
    }
    float s = s0 + s1, q = q0 + q1;
    s = dpp_add<0x128, 0xf>(s); q = dpp_add<0x128, 0xf>(q); s = dpp_add<0x124, 0xf>(s); q = dpp_add<0x124, 0xf>(q);
    s = dpp_add<0x122, 0xf>(s); q = dpp_add<0x122, 0xf>(q); s = dpp_add<0x121, 0xf>(s); q = dpp_add<0x121, 0xf>(q);
    const float mean = s * (1.f / D_), var = fmaxf(q * (1.f / D_) - mean * mean, 0.f);
    return pk_bf16(mean, sqrtf(rbf(rbf(var) + 1e-5f)));
}
template <int NG> DI void stats_poll(unsigned (&g)[NG], srd_t sws, unsigned vo, unsigned stride, unsigned tag) {
#pragma unroll 1
    for (int it = 0; it < (1 << 18); ++it) {
        bool ok = true;
#pragma unroll
        for (int k = 0; k < NG; ++k) { const u32x2 t = ld8_dev(sws, vo + stride * k, 0); g[k] = t[0]; ok &= t[1] == tag; }
        if (__all(ok)) break;
        __builtin_amdgcn_s_sleep(SL_ST);
    }
}
DI float colsum_task(const Params& p, int t, int lane) {
    const int ft = t >> 3, m = (t >> 2) & 1, qd = t & 3;
    const u32x4* src = (const u32x4*)((m ? p.wu : p.wg) + (size_t)(160 * qd + (lane >> 3)) * F_ + 64 * ft) + (lane & 7);
    u32x4 v[20];
#pragma unroll
    for (int i = 0; i < 20; ++i) v[i] = src[i * (8 * F_ / 8)];
    float c[8] = {};
#pragma unroll
    for (int i = 0; i < 20; ++i)
#pragma unroll
        for (int j = 0; j < 4; ++j) { c[2 * j] += bflo(v[i][j]); c[2 * j + 1] += bfhi(v[i][j]); }
    float o = 0.f;
#pragma unroll
    for (int j = 0; j < 8; ++j) { const float r = sum_8_16_32(c[j]); o = (lane >> 3) == j ? r : o; }
    return o;
}
DI void lnt_issue(int u, int lane, unsigned ldsaddr, srd_t sx) {
    ldsaddr = __builtin_amdgcn_readfirstlane(ldsaddr);
    const int blk = u & 31, cb = u >> 5, r4 = lane >> 4;
#pragma unroll
    for (int i = 0; i < 16; ++i) dma16(ldsaddr + i * 1024, (unsigned)(r4 * 1280 + 16 * ((lane & 15) ^ ((((i >> 1) & 3) << 2) | r4))), sx, (unsigned)((blk * 64 + 4 * i) * 1280 + cb * 256));
}
DI void lnt_finish(int u, const unsigned (&g)[8], int lane, lds_ptr lds, srd_t sws) {
    const int blk = u & 31, cb = u >> 5, gq = lane >> 4, q = (lane & 15) >> 2, pp = lane & 3, oc = 2 * gq + (pp >> 1);
    f32x2 rs[4], nm[4];
#pragma unroll
    for (int e = 0; e < 4; ++e) { rs[e] = f32x2{__builtin_amdgcn_rcpf(bfhi(g[2 * e])), __builtin_amdgcn_rcpf(bfhi(g[2 * e + 1]))}; nm[e] = -f32x2{bflo(g[2 * e]), bflo(g[2 * e + 1])} * rs[e]; }
    const unsigned ra = (8 * oc + q) * 256 + 8 * (pp & 1), fx = 16 * (((oc & 3) << 2) | q);
    lds_ptr wo = lds + (lane & 7) * 128 + 16 * ((2 * gq + ((lane >> 3) & 1)) ^ (lane & 7)), ro = lds + (lane >> 3) * 128 + 16 * ((lane & 7) ^ (lane >> 3));
    u32x2 tl[16], th[16];
#pragma unroll
    for (int k = 0; k < 16; ++k) { tl[k] = __builtin_bit_cast(u32x2, tr_read(lds + ra + ((16 * k) ^ fx))); th[k] = __builtin_bit_cast(u32x2, tr_read(lds + ra + 1024 + ((16 * k) ^ fx))); }
    WAIT_LG();
#pragma unroll
    for (int k = 0; k < 16; ++k) {
        const unsigned in[4] = {tl[k][0], tl[k][1], th[k][0], th[k][1]};
        u32x4 o;
#pragma unroll
        for (int e = 0; e < 4; ++e) { f32x2 t = __builtin_elementwise_fma(f32x2{bflo(in[e]), bfhi(in[e])}, rs[e], nm[e]); asm("" : "+v"(t)); o[e] = pk_f16(t[0], t[1]); }
        *(LAS u32x4*)(wo + k * 1024) = o;
    }
    WAIT_LG();
    u32x4 o[16];
#pragma unroll
    for (int k = 0; k < 16; ++k) o[k] = *(LAS u32x4*)(ro + k * 1024);
#pragma unroll
    for (int k = 0; k < 16; ++k) st16_dev(o[k], sws, lane * 16, WS_UT + (unsigned)(blk * D_ + cb * 128) * 128 + k * 1024);
}

struct CM { f32x2 d, n; };
DI CM cm(f32x2 w) { return CM{f32x2{w[0], w[0]}, f32x2{-w[1], w[1]}}; }
DI f32x2 cmul(f32x2 x, const CM& m) { return __builtin_elementwise_fma(__builtin_shufflevector(x, x, 1, 0), m.n, x * m.d); }
DI f32x2 cexpp(float er, float rev, float pos) {
    const float m = __builtin_amdgcn_exp2f(er * pos);
    const float t = __builtin_amdgcn_fractf(rev * pos);
    return f32x2{m * __builtin_amdgcn_cosf(t), m * __builtin_amdgcn_sinf(t)};
}
constexpr int KG_ROW = 272, KG_BYTES = 32 * KG_ROW, KT_BYTES = 1312, KT_S = 512, KT_INV = 1296;
struct KgIn { unsigned lar, aim, cre, cim, ldt, dsk; };
DI void kgen_load(KgIn& k, const Params& p, int d, int lane) {
    const int n = d * (N_ / 2) + lane;
    if (PNT) { k.lar = __builtin_nontemporal_load((const unsigned*)p.lar + n); k.aim = __builtin_nontemporal_load((const unsigned*)p.aim + n);
               k.cre = __builtin_nontemporal_load((const unsigned*)p.cre + n); k.cim = __builtin_nontemporal_load((const unsigned*)p.cim + n); }
    else { k.lar = ((const unsigned*)p.lar)[n]; k.aim = ((const unsigned*)p.aim)[n]; k.cre = ((const unsigned*)p.cre)[n]; k.cim = ((const unsigned*)p.cim)[n]; }
    const unsigned a = ((const unsigned*)p.ldt)[d >> 1], b = ((const unsigned*)p.dsk)[d >> 1];
    k.ldt = (d & 1) ? a & 0xffff0000u : a << 16; k.dsk = (d & 1) ? b & 0xffff0000u : b << 16;
}
struct KgPre { f32x2 w1, w8, w32, w256, g0; float S, invS, dk; };
DI KgPre kg_pre(const KgIn& k, int hf) {
    const float dt = rbf(expf(__uint_as_float(k.ldt)));
    const float er = (-rbf(expf(hf ? bfhi(k.lar) : bflo(k.lar))) * dt) * 1.44269504088896341f, rv = ((hf ? bfhi(k.aim) : bflo(k.aim)) * dt) * 0.15915494309189535f;
    const float bound = 2.f * dt * wave_sum(fabsf(bflo(k.cre)) + fabsf(bfhi(k.cre)) + fabsf(bflo(k.cim)) + fabsf(bfhi(k.cim))) + fabsf(__uint_as_float(k.dsk));
    int e = (int)(__float_as_uint(bound) >> 23) - 127;
    e = e < -100 ? -100 : (e > 100 ? 100 : e);
    KgPre o;
    o.S = __uint_as_float((unsigned)(127 + 13 - e) << 23); o.invS = __uint_as_float((unsigned)(127 - 13 + e) << 23); o.dk = __uint_as_float(k.dsk);
    o.g0 = f32x2{2.f * o.S * ((hf ? bfhi(k.cre) : bflo(k.cre)) * dt), 2.f * o.S * ((hf ? bfhi(k.cim) : bflo(k.cim)) * dt)};
    o.w1 = cexpp(er, rv, 1.f); o.w8 = cexpp(er, rv, 8.f); o.w32 = cexpp(er, rv, 32.f); o.w256 = cexpp(er, rv, 256.f);
    return o;
}
template <int N> DI void cmuln(f32x2 (&o)[N], const f32x2 (&x)[N], const CM (&m)[N]) {
    f32x2 t[N];
#pragma unroll
    for (int i = 0; i < N; ++i) t[i] = x[i] * m[i].d;
    SB();
#pragma unroll
    for (int i = 0; i < N; ++i) o[i] = __builtin_elementwise_fma(__builtin_shufflevector(x[i], x[i], 1, 0), m[i].n, t[i]);
    SB();
}
#define PUTB(row, g) *(LAS unsigned*)(TW + (row) * KG_ROW) = pk_f16((g)[0], (g)[1])
DI void kg_e(const KgPre& m, int lane, lds_ptr tab) {
    lds_ptr TW = tab + lane * 4;
    const CM m8 = cm(m.w8), m32 = cm(m.w32);
    f32x2 eb[4]; eb[0] = f32x2{1.f, 0.f}; eb[1] = m.w8; eb[2] = cmul(m.w8, m8); eb[3] = cmul(eb[2], m8);
    SB();
#pragma unroll
    for (int a = 0; a < 7; ++a) {
        f32x2 t[4], nx[4];
#pragma unroll
        for (int b = 0; b < 4; ++b) t[b] = eb[b] * m32.d;
        SB();
        PUTB(4 * a, eb[0]); PUTB(4 * a + 1, eb[1]); SB();
#pragma unroll
        for (int b = 0; b < 4; ++b) nx[b] = __builtin_elementwise_fma(__builtin_shufflevector(eb[b], eb[b], 1, 0), m32.n, t[b]);
        SB();
        PUTB(4 * a + 2, eb[2]); PUTB(4 * a + 3, eb[3]); SB();
#pragma unroll
        for (int b = 0; b < 4; ++b) eb[b] = nx[b];
    }
    PUTB(28, eb[0]); PUTB(29, eb[1]); PUTB(30, eb[2]); PUTB(31, eb[3]);
}
#undef PUTB
DI void kg_g(f32x2 (&g)[21], const KgPre& m) {
    const CM m1 = cm(m.w1), m256 = cm(m.w256);
    g[0] = m.g0;
    f32x2 r4[4]; cmuln<4>(r4, {m.w1, m.w256, g[0], g[0]}, {m1, m256, m1, m256});
    g[1] = r4[2]; g[8] = r4[3];
    const CM m2 = cm(r4[0]), m512 = cm(r4[1]);
    f32x2 r6[6]; cmuln<6>(r6, {r4[0], g[0], g[1], g[8], g[0], g[8]}, {m2, m2, m2, m2, m512, m512});
    g[2] = r6[1]; g[3] = r6[2]; g[9] = r6[3]; g[12] = r6[4]; g[14] = r6[5];
    const CM m4 = cm(r6[0]);
    f32x2 ra[6]; cmuln<6>(ra, {g[12], g[14], g[0], g[1], g[2], g[3]}, {m512, m512, m4, m4, m4, m4});
    g[16] = ra[0]; g[17] = ra[1]; g[4] = ra[2]; g[5] = ra[3]; g[6] = ra[4]; g[7] = ra[5];
    f32x2 rb[6]; cmuln<6>(rb, {g[16], g[17], g[8], g[9], g[12], g[14]}, {m512, m512, m4, m4, m4, m4});
    g[18] = rb[0]; g[19] = rb[1]; g[10] = rb[2]; g[11] = rb[3]; g[13] = rb[4]; g[15] = rb[5];
    g[20] = cmul(g[18], m512);
}
DI void kg_read(f16x8 (&f)[8], int lane, lds_ptr tab) {
#pragma unroll
    for (int ks = 0; ks < 8; ++ks) f[ks] = *(LAS f16x8*)(tab + (lane & 31) * KG_ROW + (lane >> 5) * 16 + ks * 32);
}
constexpr int KT_STRIDE = 1408;
DI void kg_put_table(f32x16& acc, float S, float invS, float dk, int lane, lds_ptr kt) {
    lds_ptr TS = kt + KT_S;
    const int col = lane & 31, hh = lane >> 5;
    if (lane == 0) { acc[0] += dk * S; *(LAS float*)(kt + KT_INV) = invS; }
    *(LAS u32x2*)(kt + 8 * lane) = u32x2{pk_f16(acc[3], acc[2]), pk_f16(acc[1], acc[0])};
    if (hh == 0) {
        *(LAS u32x2*)(TS + 8 * col) = u32x2{pk_f16(acc[4], acc[5]), pk_f16(acc[6], acc[7])};
#pragma unroll
        for (int i = 0; i < 4; ++i) *(LAS _Float16*)(TS + 2 * (256 + 32 * i + col)) = (_Float16)acc[8 + i];
    } else {
        *(LAS unsigned*)(TS + 2 * (128 + 2 * col)) = pk_f16(acc[4], acc[5]);
        *(LAS unsigned*)(TS + 2 * (192 + 2 * col)) = pk_f16(acc[6], acc[7]);
        if (col == 0) *(LAS unsigned*)(TS + 2 * 384) = pk_f16(acc[8], 0.f);
    }
}
DI void kg_store_table(int d, int lane, lds_ptr kt, srd_t sws) {
    const u32x4 o0 = *(LAS u32x4*)(kt + 16 * lane), o1 = *(LAS u32x4*)(kt + 1024 + 16 * (lane < 18 ? lane : 0));
    st16_dev(o0, sws, 16 * lane, WS_KT + d * KT_STRIDE);
    if (lane < 18) st16_dev(o1, sws, 16 * lane, WS_KT + d * KT_STRIDE + 1024);
}
DI void kg_half(f32x16& acc, const KgPre& m, int lane, lds_ptr tab) {
    kg_e(m, lane, tab);
    f32x2 g[21];
    kg_g(g, m);
    WAIT_LG();
    f16x8 a[8], b[8];
    kg_read(b, lane, tab);
    SB();
    unsigned gh[21];
#pragma unroll
    for (int r = 0; r < 21; ++r) gh[r] = pk_f16(g[r][0], -g[r][1]);
    WAIT_LG();
#pragma unroll
    for (int r = 0; r < 21; ++r) *(LAS unsigned*)(tab + lane * 4 + r * KG_ROW) = gh[r];
    WAIT_LG();
    kg_read(a, lane, tab);
#pragma unroll
    for (int ks = 0; ks < 8; ++ks) acc = __builtin_amdgcn_mfma_f32_32x32x16_f16(a[ks], b[ks], acc, 0, 0, 0);
    SB();
}
DI void kgen_channel(const KgIn& k, int d, int lane, lds_ptr tab, lds_ptr kt, srd_t sws) {
    f32x16 acc = {};
    float S, invS;
#pragma unroll 1
    for (int hf = 0; hf < 2; ++hf) { const KgPre m = kg_pre(k, hf); S = m.S; invS = m.invS; kg_half(acc, m, lane, tab); }
    kg_put_table(acc, S, invS, __uint_as_float(k.dsk), lane, kt);
    WAIT_LG();
    kg_store_table(d, lane, kt, sws);
}

constexpr int CV_COPY = 4160, CV_U = 4 * CV_COPY, CV_BYTES = CV_U + 4608;
struct ConvIn { u32x4 uu[4]; u32x2 z, a[3]; unsigned inv; };
DI unsigned addh2(unsigned a, unsigned b) { return __builtin_bit_cast(unsigned, __builtin_bit_cast(f16x2, a) + __builtin_bit_cast(f16x2, b)); }
DI void conv_local_k(ConvIn& c, int lane, lds_ptr kt) {
    const u32x2 z0 = *(LAS u32x2*)(kt + 8 * lane), z1 = *(LAS u32x2*)(kt + KT_BYTES + 8 * lane);
    c.z = u32x2{addh2(z0[0], z1[0]), addh2(z0[1], z1[1])};
#pragma unroll
    for (int sg = 0; sg < 3; ++sg) { lds_ptr q = kt + KT_S + 256 * sg + 4 * lane;
        c.a[sg] = u32x2{addh2(*(LAS unsigned*)q, *(LAS unsigned*)(q + KT_BYTES)), addh2(*(LAS unsigned*)(q + 4), *(LAS unsigned*)(q + KT_BYTES + 4))}; }
    c.inv = *(LAS unsigned*)(kt + KT_INV);
}
DI void conv_load(ConvIn& c, int d, int lane, srd_t sws, bool remote_k) {
    const unsigned U = WS_UT + (unsigned)d * 128, vu = (lane >> 3) * (D_ * 128) + (lane & 7) * 16, T = WS_KT + d * KT_STRIDE;
    if (remote_k) {
        c.z = ld8_dev(sws, 8 * lane, T);
#pragma unroll
        for (int sg = 0; sg < 3; ++sg) c.a[sg] = ld8_dev(sws, 4 * lane, T + KT_S + 256 * sg);
        c.inv = __builtin_amdgcn_raw_buffer_load_b32(sws, 0, T + KT_INV, SC1);
    }
#pragma unroll
    for (int i = 0; i < 4; ++i) c.uu[i] = ld16_dev(sws, vu, U + i * (8 * D_ * 128));
}
DI void conv_stage_k(const ConvIn& c, int lane, lds_ptr lds) {
    const unsigned a0[3] = {c.a[0][0], c.a[1][0], c.a[2][0]}, a1[3] = {c.a[0][1], c.a[1][1], c.a[2][1]};
    if (lane < 30) *(LAS u32x4*)(lds + CV_U + 16 * lane) = u32x4{0, 0, 0, 0};
    else if (lane < 46) *(LAS u32x4*)(lds + ((lane - 30) >> 2) * CV_COPY + 4096 + 16 * ((lane - 30) & 3)) = u32x4{0, 0, 0, 0};
    *(LAS u32x2*)(lds + 2 * (2044 - 8 * (lane & 31) - 4 * (lane >> 5))) = c.z;
#define PK(sg, lo, hi) __builtin_amdgcn_perm(a1[sg], a0[sg], (unsigned)((2 * (lo)) | ((2 * (lo) + 1) << 8) | ((2 * (hi)) << 16) | ((2 * (hi) + 1) << 24)))
    *(LAS u32x2*)(lds + 2 * (1788 - 4 * lane)) = u32x2{PK(0, 2, 1), PK(0, 0, 0)};
    *(LAS u32x4*)(lds + 2 * (1528 - 8 * lane)) = u32x4{PK(1, 2, 2), PK(1, 1, 1), PK(1, 1, 0), PK(1, 0, 0)};
    { const unsigned t22 = PK(2, 2, 2), t11 = PK(2, 1, 1), t10 = PK(2, 1, 0), t00 = PK(2, 0, 0);
      *(LAS u32x4*)(lds + 2 * (1008 - 16 * lane)) = u32x4{t22, t22, t11, t11};
      *(LAS u32x4*)(lds + 2 * (1008 - 16 * lane + 8)) = u32x4{t11, t10, t00, t00}; }
#undef PK
    WAIT_LG();
    u32x4 e4[4]; u32x2 e2[4];
#pragma unroll
    for (int i = 0; i < 4; ++i) { e4[i] = *(LAS u32x4*)(lds + 1024 * i + 16 * lane); e2[i] = *(LAS u32x2*)(lds + 1024 * i + 16 * lane + 16); SB(); }
#pragma unroll
    for (int i = 0; i < 4; ++i) {
        const u32x4 e = e4[i]; const u32x2 f = e2[i];
        const unsigned o0 = __builtin_amdgcn_alignbit(e[1], e[0], 16), o1 = __builtin_amdgcn_alignbit(e[2], e[1], 16), o2 = __builtin_amdgcn_alignbit(e[3], e[2], 16),
                       o3 = __builtin_amdgcn_alignbit(f[0], e[3], 16), o4 = __builtin_amdgcn_alignbit(f[1], f[0], 16);
        lds_ptr q = lds + 1024 * i + 16 * lane;
        *(LAS u32x4*)(q + CV_COPY) = u32x4{o0, o1, o2, o3};
        *(LAS u32x4*)(q + 2 * CV_COPY) = u32x4{e[1], e[2], e[3], f[0]};
        *(LAS u32x4*)(q + 3 * CV_COPY) = u32x4{o1, o2, o3, o4};
    }
}
DI void conv_stage_u(const ConvIn& c, int lane, lds_ptr lds) {
#pragma unroll
    for (int i = 0; i < 4; ++i) *(LAS u32x4*)(lds + CV_U + 480 + 1024 * i + 16 * lane) = c.uu[i];
    WAIT_LG();
}
DI constexpr int cv_dl(int m) { return m < 8 ? 0 : m < 15 ? 1 : m < 21 ? 2 : m < 26 ? 3 : m < 30 ? 4 : m < 33 ? 5 : m < 35 ? 6 : 7; }
DI constexpr int cv_cb(int m) { const int st[8] = {0, 8, 15, 21, 26, 30, 33, 35}; return 7 - cv_dl(m) - (m - st[cv_dl(m)]); }
DI constexpr int cv_ra(int m) { return m < 2 ? 1 : m == 5 || m == 6 ? 2 : m == 11 || m == 12 ? 3 : m == 17 || m == 18 ? 4 : m == 19 || m == 20 ? 5 : m == 23 || m == 24 ? 6 : m == 25 || m == 26 ? 7 : m == 28 || m == 29 ? 8 : -1; }
DI constexpr int cv_rh(int m) { return m == 1 || m == 6 || m == 12 || m == 18 || m == 20 || m == 24 || m == 26 || m == 29; }
DI constexpr int cv_rb(int m) { return m == 2 ? 7 : m == 9 ? 6 : m == 16 ? 5 : m == 22 ? 4 : m == 27 ? 3 : m == 31 ? 2 : m == 34 ? 1 : m == 35 ? 0 : -1; }
struct ConvRegs { u32x2 al[8], ah[8]; f16x8 b[8]; f32x4 acc[8]; };
template <int S, int M> DI void cv_slot(ConvRegs& r, lds_ptr pa, lds_ptr pb) {
    constexpr int dl = cv_dl(M), cb = cv_cb(M), ra = cv_ra(M), rb = cv_rb(M);
    r.acc[dl + cb] = __builtin_amdgcn_mfma_f32_16x16x32_f16(__builtin_bit_cast(f16x8, u32x4{r.al[dl][0], r.al[dl][1], r.ah[dl][0], r.ah[dl][1]}), r.b[cb], r.acc[dl + cb], 0, 0, 0);
    if constexpr (ra >= 1 && ra <= 7) { if constexpr (cv_rh(M)) r.ah[ra] = *(LAS u32x2*)(pa + 2 * (256 * (7 - ra) + 32 * (7 - S)) + 8); else r.al[ra] = *(LAS u32x2*)(pa + 2 * (256 * (7 - ra) + 32 * (7 - S))); }
    if constexpr (ra == 8 && S < 7) { if constexpr (cv_rh(M)) r.ah[0] = *(LAS u32x2*)(pa + 2 * (256 * 7 + 32 * (6 - S)) + 8); else r.al[0] = *(LAS u32x2*)(pa + 2 * (256 * 7 + 32 * (6 - S))); }
    if constexpr (rb >= 0 && S < 7) r.b[rb] = *(LAS f16x8*)(pb + 2 * (256 * rb + 32 * (6 - S)));
    SB();
}
template <int S, int... M> DI void cv_step(ConvRegs& r, lds_ptr pa, lds_ptr pb, std::integer_sequence<int, M...>) { (cv_slot<S, M>(r, pa, pb), ...); }
template <int... S> DI void cv_steps(ConvRegs& r, lds_ptr pa, lds_ptr pb, std::integer_sequence<int, S...>) { (cv_step<S>(r, pa, pb, std::make_integer_sequence<int, 36>{}), ...); }
DI void conv_run(int d, float inv, int lane, lds_ptr lds, srd_t sws) {
    const int b = lane & 15, h = lane >> 4;
    lds_ptr pa = lds + (3 - (b & 3)) * CV_COPY + 2 * (2052 - 8 * h - 4 * (b >> 2)) - 2 * (256 * 7 + 32 * 7);
    lds_ptr pb = lds + CV_U + 2 * (240 + 16 * b - 8 * h + 8) - 2 * 32 * 7;
    ConvRegs r;
#pragma unroll
    for (int c = 0; c < 8; ++c) r.acc[c] = f32x4{0.f, 0.f, 0.f, 0.f};
    r.al[0] = *(LAS u32x2*)(pa + 2 * (256 * 7 + 32 * 7)); SB(); r.ah[0] = *(LAS u32x2*)(pa + 2 * (256 * 7 + 32 * 7) + 8); SB();
#pragma unroll
    for (int c = 7; c >= 0; --c) r.b[c] = *(LAS f16x8*)(pb + 2 * (256 * c + 32 * 7));
    SB();
    cv_steps(r, pa, pb, std::make_integer_sequence<int, 8>{});
    const unsigned S = WS_ST + (unsigned)d * 128, vs = (b >> 2) * (D_ * 128) + (b & 3) * 32 + 8 * h;
#pragma unroll
    for (int c = 0; c < 8; ++c) st8_dev(u32x2{pk_bf16(r.acc[c][0] * inv, r.acc[c][1] * inv), pk_bf16(r.acc[c][2] * inv, r.acc[c][3] * inv)}, sws, vs, S + c * (4 * D_ * 128));
}

template <int NP = 8> DI void wd_issue(int u, int lane, unsigned ldsaddr, srd_t swd, int i0 = 0) {
    ldsaddr = __builtin_amdgcn_readfirstlane(ldsaddr);
    const int fs = u / 5, cb = u % 5;
#pragma unroll
    for (int ii = 0; ii < NP; ++ii) { const int i = i0 + ii, row = 4 * i + (lane >> 4), p16 = lane & 15;
        dma16_nt(ldsaddr + i * 1024, (unsigned)(row * 1280 + 16 * ((((p16 >> 1) ^ (row & 7)) << 1) | (p16 & 1))), swd, (unsigned)(fs * 32 * 1280 + cb * 256)); }
}
template <int NP = 8> DI void wd_finish(int u, int lane, lds_ptr lds, srd_t sws, int i0 = 0) {
    const int fs = u / 5, cb = u % 5;
    const int kq = lane >> 4, q = (lane & 15) >> 2, pp = lane & 3, row = 4 * kq + q;
    u32x2 ta[NP], tb[NP];
#pragma unroll
    for (int i = 0; i < NP; ++i) { ta[i] = __builtin_bit_cast(u32x2, tr_read(lds + row * 256 + 32 * ((i0 + i) ^ (row & 7)) + 8 * pp));
                                   tb[i] = __builtin_bit_cast(u32x2, tr_read(lds + (16 + row) * 256 + 32 * ((i0 + i) ^ (row & 7)) + 8 * pp)); }
#pragma unroll
    for (int i = 0; i < NP; ++i) {
        const int d16 = 8 * cb + i0 + i;
        st16_dev(u32x4{ta[i][0], ta[i][1], tb[i][0], tb[i][1]}, sws, lane * 16, WS_WD + (unsigned)(((d16 / 5) * 54 + fs) * 5 + d16 % 5) * 1024);
    }
    WAIT_LG();
}

constexpr int G1_SLOT = 49152, G1_WG = 32768, G1_WU = 40960;
constexpr int AUX_W = LDS_BYTES - 128, AUX_GO = AUX_W, AUX_TAG = AUX_W + 4, AUX_KC = AUX_W + 16, AUX_DONE = AUX_W + 32, AUX_RS = AUX_W + 64;
constexpr int G2_RING = 3 * CV_BYTES, G2_HALF = 15360;
constexpr int AUX_CS = G2_RING + 6 * G2_HALF, AUX_ST = AUX_CS + 1024;
constexpr int g2_slot(int st) { return (st % 5) < 3 ? G2_RING + (st % 5) * 2 * G2_HALF : ((st % 5) - 3) * 2 * G2_HALF; }
constexpr int G2_S = g2_slot(4), G2_X = G2_S + 10240, G2_O = g2_slot(6);
constexpr int KG_BASE = 2 * G1_SLOT, KT_BASE = AUX_W - 4 * KT_BYTES;
static_assert(AUX_ST + 1024 <= KT_BASE, "LDS map");
constexpr int WD_TMP = 3 * G1_SLOT;
constexpr int HW_BASE = 65536, HK_BASE = 65536 + 16384;
static_assert(AUX_ST + 1024 <= AUX_W && HK_BASE + 8 * (KG_BYTES + KT_BYTES) <= AUX_W, "LDS map");
struct Frag { u32x4 x[4]; s16x4 wl[4], wh[4]; };
#define MF16(acc, lo, hi, X) acc = __builtin_amdgcn_mfma_f32_16x16x32_bf16(__builtin_bit_cast(bf16x8, s16x8{lo[0], lo[1], lo[2], lo[3], hi[0], hi[1], hi[2], hi[3]}), __builtin_bit_cast(bf16x8, X), acc, 0, 0, 0)
template <int NV> DI void wait_h(u32x4& a) { asm volatile("s_waitcnt vmcnt(%1)" : "+v"(a) : "n"(NV) : "memory"); }
template <int I> using IC = std::integral_constant<int, I>;
struct G2Regs { u32x4 hb[HD]; bf16x8 w[2][5]; f32x4 acc[5]; };
template <int T> DI void g2_step(G2Regs& r, lds_ptr pw, int lane, srd_t sws, unsigned soh) {
    constexpr int nx = T + 1;
    if constexpr (T % 3 == 2 && T != 5 && T < 26) { LBAR(); SB(); }
    wait_h<(T + HD <= 27 ? HD - 1 : 26 - T)>(r.hb[T % HD]); SB();
#pragma unroll
    for (int j = 0; j < 5; ++j) {
        r.acc[j] = __builtin_amdgcn_mfma_f32_16x16x32_bf16(r.w[T & 1][j], __builtin_bit_cast(bf16x8, r.hb[T % HD]), r.acc[j], 0, 0, 0);
        if constexpr (nx < 27) r.w[nx & 1][j] = *(LAS bf16x8*)(pw + g2_slot(nx / 3) + ((nx % 3) * 5 + j) * 1024);
        SB();
    }
    if constexpr (T + HD < 27) { hload(r.hb[T % HD], lane * 16, sws, soh + (T + HD) * 4096); SB(); }
}
template <int B, int... T> DI void g2_steps(G2Regs& r, lds_ptr pw, int lane, srd_t sws, unsigned soh, std::integer_sequence<int, T...>) { (g2_step<B + T>(r, pw, lane, sws, soh), ...); }

__global__ __launch_bounds__(768, 3) void fused_kernel(Params p) {
    extern __shared__ __attribute__((aligned(16))) char smem[];
    lds_ptr lds = (lds_ptr)smem;
    const unsigned lds0 = (unsigned)(uintptr_t)lds;
    const int tid = threadIdx.x, lane = tid & 63, w = __builtin_amdgcn_readfirstlane(tid >> 6);
    const int xg = blockIdx.x & 7, bi = blockIdx.x >> 3;
    const bool gemm_wg = bi >= 5;
    syp sy = (syp)(p.ws + WS_SYNC);
    const srd_t sws = mk_srd(p.ws, WS_END);
    STAMP_DECL; STAMP(0);
    const int rt = 4 * xg + (bi & 3), dt = bi >> 2;
    const unsigned vl16 = lane * 16;
    auto w2_src = [&](int st, int pc) { return WS_WD + (unsigned)((dt * 54 + 3 * st) * 5 + pc + (pc >= 15 ? 27 * 5 - 15 : 0)) * 1024; };
    auto w2_stage = [&](int st, int first, int step, int np) {
#pragma unroll
        for (int i = 0; i < np; ++i) { const int pc = first + step * i; dma16_dev(lds0 + g2_slot(st) + pc * 1024, vl16, sws, w2_src(st, pc)); } };
#define LOAD_E(v) asm volatile("global_load_dword %0, %1, %2 sc1" : "=v"(v) : "v"(0u), "s"(p.ws + WS_SYNC + 128 * SY_E) : "memory")
#define TAKE_TAG(v, N) do { asm volatile("s_waitcnt vmcnt(" #N ")\n\tv_readfirstlane_b32 %0, %1\n\ts_nop 4" : "=s"(tag) : "v"(v) : "memory"); tag += 1u; } while (0)
#define READ_TAG() (tag = __builtin_amdgcn_readfirstlane(lds_ld(lds + AUX_TAG)))
    unsigned tag;
    int s_pending = -1;
    if (gemm_wg) {
        const int t = (xg & 3) * 27 + (bi - 5), panel = (xg >> 2) * 4 + (t & 3), ft = t >> 2;
        const int ch0 = 80 * xg + 3 * (bi - 5), nch = bi == 31 ? 2 : 3, gidx = (bi - 5) * 8 + xg;
        if (w >= 8) {
            const int lw = w - 8;
            const srd_t sx = mk_srd(p.x, L_ * D_ * 2), sg = mk_srd(p.wg, D_ * F_ * 2), su = mk_srd(p.wu, D_ * F_ * 2);
            const unsigned vx0 = (lane >> 3) * 1280 + 16 * ((lane & 7) ^ (lane >> 4)), vx1 = vx0 ^ 64;
            const unsigned vw0 = (lane >> 3) * 3456 + 16 * ((lane & 7) ^ (2 * ((lane >> 4) & 1))), vw1 = vw0 ^ 64;
            const unsigned sox = (unsigned)(panel * 256 + 64 * lw) * 1280, sow = (unsigned)(16 * lw) * 3456 + ft * 128;
            const int rho = ROT1 == 2 ? (3 * (ft + panel)) % 10 : ROT1 == 3 ? (3 * ft + 5 * (panel >> 2)) % 10 : ROT1 == 4 ? (5 * ((ft + panel) & 1) + ((xg & 3) * 5 + 1) / 2) % 10 : ROT1 ? ((xg & 3) * 5 + 1) / 2 + 5 * (xg >> 2) : 0;
            auto tile = [&](int k0, unsigned slot, int p0 = 0, int p1 = 12) {
                int kt = k0 + rho; kt -= kt >= 20 ? 20 : kt >= 10 ? 10 : 0;
#pragma unroll
                for (int i = p0; i < p1; ++i) {
                    if (i < 4) dma16(lds0 + slot + ((i & 1) ? G1_WU : G1_WG) + lw * 2048 + (i >> 1) * 1024, (i >> 1) ? vw1 : vw0, (i & 1) ? su : sg, sow + kt * 64 * 3456 + (i >> 1) * 8 * 3456);
                    else dma16(lds0 + slot + (8 * lw + i - 4) * 1024, (i & 1) ? vx1 : vx0, sx, sox + (i - 4) * 8 * 1280 + kt * 128);
                }
            };
            if (GSLEEP) __builtin_amdgcn_s_sleep(GSLEEP);
            asm volatile("s_nop 4" ::: "memory");
            tile(0, 0); tile(1, G1_SLOT);
            auto touch = [&](int k0) { unsigned t_; int kt = k0 + rho; kt -= kt >= 20 ? 20 : kt >= 10 ? 10 : 0;
              const int xr = (ft * 256) / 27 + (lane < 10 ? lane : 0);
              const char* a = lane < 10 || lane >= TLANES ? (const char*)p.x + (size_t)(panel * 256 + (xr < 256 ? xr : 255)) * 1280 + kt * 128
                                        : (const char*)(lane < 18 ? p.wg : p.wu) + (size_t)(kt * 64 + 8 * panel + ((lane - 10) & 7)) * 3456 + ft * 128;
              asm volatile("global_load_dword %0, %1, off" : "=v"(t_) : "v"(a) : "memory"); return t_; };
            const unsigned tch = touch(2 + lw);
            WAIT_BAR(13);
            tile(2, 2 * G1_SLOT);
            WAIT_BAR(13);
            tile(3, 0);
            wd_issue<2>(gidx, lane, lds0 + WD_TMP, mk_srd(p.wd, D_ * F_ * 2), 2 * lw);
            const unsigned tch2 = touch(6 + lw);
            READ_TAG();
            if (lw == 0) { if (lane == 0) sy_st(sy + SY_ACK + blockIdx.x, tag); WAIT_BAR(16); } else WAIT_BAR(15);
            asm volatile("" :: "v"(tch));
            tile(4, G1_SLOT);
            WAIT_BAR(12);
            asm volatile("" :: "v"(tch2));
            tile(5, 2 * G1_SLOT);
            wd_finish<2>(gidx, lane, lds + WD_TMP, sws, 2 * lw);
            WAIT_BAR(14);
            tile(6, 0);
            WAIT_BAR(12);
            tile(7, G1_SLOT);
            if (lw == 3) { if (lane == 0) sy_st(sy + SY_WD + gidx, tag); WAIT_BAR(13); } else WAIT_BAR(12);
            tile(8, 2 * G1_SLOT);
            WAIT_BAR(12);
            const int ch = ch0 + lw;
            const bool cv = lw < nch;
            tile(9, 0);
            WAIT_BAR(12);
            const unsigned vpf = 128u * (!cv ? SY_ALL + xg : lane >= 32 && lw >= NGK ? SY_K + ch : SY_LN + 32 * (ch >> 7) + (lane & 31));
            unsigned pf;
            asm volatile("buffer_load_dword %0, %1, %2, %3 offen sc1" : "=v"(pf) : "v"(vpf), "s"(sws), "s"(WS_SYNC) : "memory");
            asm volatile("s_waitcnt vmcnt(0) lgkmcnt(0)\n\ts_barrier" : "+v"(pf) :: "memory");
            if (lw == 3) {
                const bool wd_ready = __all(pf == tag);
                if (wd_ready && WMODE != 4 && WMODE != 6) w2_stage(0, 0, 1, 30);
                LBAR();
                STAMP(1);
                if (!wd_ready) poll_all<1, 4>(sy + SY_ALL + xg, 1, tag, lane);
                if (WMODE == 4 || WMODE == 6) lds_wait(lds + AUX_DONE, 8, 1, lane);
                if (!wd_ready || WMODE == 4 || WMODE == 6) w2_stage(0, 0, 1, 30);
                if (WMODE == 5 || WMODE == 6) { WAIT_VM(0); if (lane == 0) lds_st(lds + AUX_GO + 8, 1u); lds_wait(lds + AUX_GO, 1, 1, lane); }
                if (WMODE == 0) lds_wait(lds + AUX_DONE, 8, 1, lane);
                if (WMODE == 3) { if (lane == 0) lds_st(lds + AUX_KC + 12, 1u); lds_wait(lds + AUX_KC, 4, 1, lane); }
                w2_stage(1, 0, 1, 30);
                WAIT_VM(30);
                if (WMODE == 1) lds_wait(lds + AUX_DONE, 8, 1, lane);
                if (lane == 0) lds_st(lds + AUX_GO + 8, 1u);
                w2_stage(2, 0, 1, 30);
                STAMP(2);
            } else {
                const bool loaded = cv && !NOCONV && __all(pf == tag);
                ConvIn c;
                {
                  unsigned g[4];
                  stats_poll<4>(g, sws, lw == 2 ? WS_STATS + panel * 2048 + lane * 32 : WS_CS + (ft * 8 + 4 * lw) * 512 + lane * 8, lw == 2 ? 8 : 512, tag);
                  if (loaded) conv_load(c, ch, lane, sws, lw >= NGK);
                  if (lw == 2) *(LAS u32x4*)(lds + AUX_ST + lane * 16) = u32x4{g[0], g[1], g[2], g[3]};
                  else *(LAS float*)(lds + AUX_CS + lw * 256 + lane * 4) = (__uint_as_float(g[0]) + __uint_as_float(g[1])) + (__uint_as_float(g[2]) + __uint_as_float(g[3])); }
                LBAR();
                STAMP(1);
                if (WMODE == 3 && (!cv || NOCONV) && lane == 0) lds_st(lds + AUX_KC + 4 * lw, 1u);
                if (cv && !NOCONV) {
                    lds_ptr cs = lds + lw * CV_BYTES;
                    if (!loaded) {
                        if (WMODE == 3 && lane == 0) lds_st(lds + AUX_KC + 4 * lw, 1u);
#pragma unroll 1
                        for (int it = 0; it < (1 << 18); ++it) { if (__all(sy_ld(sy + (vpf >> 7)) == tag)) break; __builtin_amdgcn_s_sleep(SL_K); }
                        asm volatile("" ::: "memory");
                        conv_load(c, ch, lane, sws, lw >= NGK); }
                    if (lw < NGK) conv_local_k(c, lane, lds + KT_BASE + 2 * lw * KT_BYTES);
                    STAMP(2);
                    __builtin_amdgcn_s_setprio(STPRIO);
                    conv_stage_k(c, lane, cs);
                    conv_stage_u(c, lane, cs);
                    if (WMODE == 3 && lane == 0) lds_st(lds + AUX_KC + 4 * lw, 1u);
                    __builtin_amdgcn_s_setprio(0);
                    if (CVWAIT) lds_wait(lds + AUX_DONE, 8, CVWAIT, lane);
                    STAMP(3);
                    conv_run(ch, __uint_as_float(c.inv), lane, cs, sws);
                    STAMP(4);
                }
                s_pending = cv ? ch : -1;
                STAMP(5);
            }
        } else {
            if (w == 7 && lane < 16 && lane != 1) *(LAS unsigned*)(lds + AUX_W + 4 * lane) = 0u;
            if (w < 2 * NGK) {
                KgIn k; kgen_load(k, p, ch0 + (w >> 1), lane);
                const KgPre m = kg_pre(k, w & 1);
                f32x16 acc = {};
                kg_half(acc, m, lane, lds + KG_BASE + w * KG_BYTES);
                kg_put_table(acc, m.S, m.invS, (w & 1) ? 0.f : m.dk, lane, lds + KT_BASE + w * KT_BYTES);
            } else if (w >= 5 && (w < 7 || gidx < 160)) {
                const int r = 4 * (w - 5) + (lane >> 4), rr = r < 10 ? r : 9, row = rr < 8 ? 8 * gidx + rr : 1728 + 2 * gidx + rr - 8;
                unsigned e_raw;
                LOAD_E(e_raw);
                const unsigned rs = row_stats4(p.x, row, lane);
                TAKE_TAG(e_raw, 0);
                if ((lane & 15) == 0 && r < 10) __hip_atomic_store((GAS unsigned long long*)(p.ws + WS_STATS) + row, ((unsigned long long)tag << 32) | rs, __ATOMIC_RELAXED, __HIP_MEMORY_SCOPE_AGENT);
                if (w == 5 && lane == 0) lds_st(lds + AUX_TAG, tag);
            }
            STAMP(15);
            const int tg = w >> 1, fg = w & 1, kq = lane >> 4, l16 = lane & 15;
            unsigned ax[2], aw[2];
            { const int row = 64 * tg + l16, q = l16 >> 2, pp = lane & 3;
#pragma unroll
              for (int ks = 0; ks < 2; ++ks) {
                  ax[ks] = row * 128 + 16 * ((4 * ks + kq) ^ ((row >> 1) & 7));
                  aw[ks] = G1_WG + (32 * ks + 8 * kq + q) * 128 + 32 * ((2 * fg) ^ ((q >> 1) | ((kq & 1) << 1))) + 8 * pp; } }
            f32x4 ag[4][2] = {}, au[4][2] = {};
            Frag A, B;
#define RX(N, sl, ks, i) N.x[i] = *(LAS u32x4*)(lds + (sl) + ax[ks] + (i) * 2048)
#define RWL(N, sl, ks, j) N.wl[j] = tr_read(lds + (sl) + (aw[ks] ^ (((j) & 1) * 32)) + ((j) >> 1) * 8192)
#define RWH(N, sl, ks, j) N.wh[j] = tr_read(lds + (sl) + (aw[ks] ^ (((j) & 1) * 32)) + ((j) >> 1) * 8192 + 512)
#define MG(C, i, j) MF16(ag[i][j], C.wl[j], C.wh[j], C.x[i])
#define MU(C, i, j) MF16(au[i][j], C.wl[2 + j], C.wh[2 + j], C.x[i])
#define G_(M, RD, R) do { M; if (RD) { R; } SB(); } while (0)
#define STEP(C, N, RD, sl, ks) do { \
        G_(MG(C, 0, 0), RD, RX(N, sl, ks, 0));  G_(MU(C, 0, 0), RD, RWL(N, sl, ks, 0)); G_(MG(C, 0, 1), RD, RWH(N, sl, ks, 0)); G_(MU(C, 0, 1), RD, RWL(N, sl, ks, 2)); \
        G_(MG(C, 1, 0), RD, RWH(N, sl, ks, 2)); G_(MU(C, 1, 0), RD, RWL(N, sl, ks, 1)); G_(MG(C, 1, 1), RD, RWH(N, sl, ks, 1)); G_(MU(C, 1, 1), RD, RWL(N, sl, ks, 3)); \
        G_(MG(C, 2, 0), RD, RWH(N, sl, ks, 3)); G_(MU(C, 2, 0), RD, RX(N, sl, ks, 1));  G_(MG(C, 2, 1), RD, RX(N, sl, ks, 2));  G_(MU(C, 2, 1), RD, RX(N, sl, ks, 3)); \
        G_(MG(C, 3, 0), 0, (void)0); G_(MU(C, 3, 0), 0, (void)0); G_(MG(C, 3, 1), 0, (void)0); G_(MU(C, 3, 1), 0, (void)0); } while (0)
            LBAR();
            STAMP(1);
            RX(A, 0, 0, 0); RWL(A, 0, 0, 0); RWH(A, 0, 0, 0); RWL(A, 0, 0, 2); RWH(A, 0, 0, 2); RWL(A, 0, 0, 1); RWH(A, 0, 0, 1); RWL(A, 0, 0, 3); RWH(A, 0, 0, 3);
            RX(A, 0, 0, 1); RX(A, 0, 0, 2); RX(A, 0, 0, 3);
#pragma unroll
            for (int kt = 0; kt < 9; ++kt) {
                STEP(A, B, 1, (kt % 3) * G1_SLOT, 1);
                LBAR(); SB();
                STEP(B, A, 1, ((kt + 1) % 3) * G1_SLOT, 0);
            }
            STEP(A, B, 1, 0, 1);
            STEP(B, A, 0, 0, 0);
            STAMP(2);
            LBAR();
            __builtin_amdgcn_s_setprio(1);
            READ_TAG();
            f32x4 cgv[2], cuv[2];
#pragma unroll
            for (int j = 0; j < 2; ++j) { lds_ptr q = lds + AUX_CS + (32 * fg + 16 * j + 4 * kq) * 4; cgv[j] = *(LAS f32x4*)q; cuv[j] = *(LAS f32x4*)(q + 256); }
            unsigned stt[4];
#pragma unroll
            for (int i = 0; i < 4; ++i) stt[i] = *(LAS unsigned*)(lds + AUX_ST + (64 * tg + 16 * i + l16) * 4);
            const float K0 = -2.f * 0.7978845608028654f * 1.44269504088896341f, K1 = K0 * 0.044715f;
#pragma unroll
            for (int i = 0; i < 4; ++i) {
                const float ir = bfhi(stt[i]), r = __builtin_amdgcn_rcpf(ir);
                const f32x2 nm = {-bflo(stt[i]), -bflo(stt[i])}, c0 = {K0 * r, K0 * r}, c1 = {K1 * r * r * r, K1 * r * r * r}, ir2 = {ir * ir, ir * ir};
                u32x4 o;
#pragma unroll
                for (int e = 0; e < 4; ++e) {
                    const int j = e >> 1, b = 2 * (e & 1);
                    const f32x2 g1 = nm * f32x2{cgv[j][b], cgv[j][b + 1]} + f32x2{ag[i][j][b], ag[i][j][b + 1]};
                    const f32x2 u1 = nm * f32x2{cuv[j][b], cuv[j][b + 1]} + f32x2{au[i][j][b], au[i][j][b + 1]};
                    const f32x2 z = g1 * (g1 * g1 * c1 + c0);
                    const f32x2 ex = {__builtin_amdgcn_exp2f(z[0]), __builtin_amdgcn_exp2f(z[1])};
                    const f32x2 dd = ex * ir2 + ir2;
                    const f32x2 hv = g1 * u1 * f32x2{__builtin_amdgcn_rcpf(dd[0]), __builtin_amdgcn_rcpf(dd[1])};
                    o[e] = pk_bf16(hv[0], hv[1]);
                }
                if (CVWAIT == 1 && i == 3 && lane == 0) lds_st(lds + AUX_DONE + 4 * w, 1u);
                st16_h(o, sws, vl16, WS_H + (unsigned)(((panel * 4 + tg) * 54 + ft * 2 + fg) * 4 + i) * 1024);
            }
            if (lane == 0) lds_st(lds + AUX_DONE + 4 * w, 2u);
            STAMP(3);
            publish(sy + SY_H + 64 * (4 * panel + tg) + 2 * ft + fg, tag, lane);
            STAMP(4);
        }
    } else {
        const int hi = bi * 8 + xg;
        unsigned e_raw;
        if (w < 8) {
            constexpr int PER = NGK == 1 ? 53 : 26, NREM = 8 * PER;
            auto chan = [](int v) { const int g = v / PER, r = v % PER; return 80 * g + (NGK == 1 ? 3 * (r >> 1) + 1 + (r & 1) : 3 * r + 2); };
            const int v0 = w * 40 + hi, v1 = v0 + 320;
            LOAD_E(e_raw);
            if (tid < 16) *(LAS unsigned*)(lds + AUX_W + 4 * tid) = 0u;
            if (v0 >= NREM) {
#pragma unroll 1
                for (int t = v0 - NREM; t < 216; t += 320 - NREM) { const float cs = colsum_task(p, t, lane); TAKE_TAG(e_raw, 0);
                    __hip_atomic_store((GAS unsigned long long*)(p.ws + WS_CS) + t * 64 + 8 * (lane & 7) + (lane >> 3), ((unsigned long long)tag << 32) | __float_as_uint(cs), __ATOMIC_RELAXED, __HIP_MEMORY_SCOPE_AGENT); }
            } else {
                KgIn k0, k1;
                kgen_load(k0, p, chan(v0), lane);
                if (NGK == 1) kgen_load(k1, p, chan(v1 < NREM ? v1 : 0), lane);
                TAKE_TAG(e_raw, 0);
                STAMP(1);
#pragma unroll 1
                for (int v = v0; v < NREM; v += 320) {
                    kgen_channel(k0, chan(v), lane, lds + HK_BASE + w * KG_BYTES, lds + HK_BASE + 8 * KG_BYTES + w * KT_BYTES, sws);
                    publish(sy + SY_K + chan(v), tag, lane);
                    if (NGK == 1) k0 = k1;
                }
            }
            STAMP(4);
        } else {
            const int aw = hi * 4 + (w - 8), off = (w - 8) * 16384, wb = 216 + 2 * hi + (w - 8);
            const bool wd = w < 10 && wb < 270;
            const unsigned long long t_start = __builtin_amdgcn_s_memtime();
            LOAD_E(e_raw);
            __builtin_amdgcn_s_sleep(HSLEEP);
            __builtin_amdgcn_s_setprio(HPRIO);
            asm volatile("s_nop 4" ::: "memory");
            lnt_issue(aw, lane, lds0 + off, mk_srd(p.x, L_ * D_ * 2));
            if (wd) { wd_issue(wb, lane, lds0 + HW_BASE + (w - 8) * 8192, mk_srd(p.wd, D_ * F_ * 2)); WAIT_VM(0); wd_finish(wb, lane, lds + HW_BASE + (w - 8) * 8192, sws); }
            TAKE_TAG(e_raw, 0);
            if (wd && lane == 0) sy_st(sy + SY_WD + wb, tag);
            STAMP(2);
            while (__builtin_amdgcn_s_memtime() - t_start < STDELAY) __builtin_amdgcn_s_sleep(4);
            unsigned g[8];
            stats_poll<8>(g, sws, WS_STATS + (aw & 31) * 512 + (2 * (lane >> 4) + ((lane >> 3) & 1)) * 64, 8, tag);
            WAIT_VM(0);
            STAMP(3);
            lnt_finish(aw, g, lane, lds + off, sws);
            STAMP(6);
            publish(sy + SY_LN + aw, tag, lane);
            STAMP(4);
            if (w == 10 && bi == 4) { poll_all<5, 8>(sy + SY_WD, 270, tag, lane); if (lane == 0) sy_st(sy + SY_ALL + xg, tag); }
            if (w == 11) poll_all<1, 16>(sy + SY_ALL + xg, 1, tag, lane);
            __builtin_amdgcn_s_setprio(0);
        }
        LBAR();
        if (w == 9 && lane == 0) sy_st(sy + SY_ACK + blockIdx.x, tag);
        if (w == 10 && hi == 39) { poll_all<4, 64>(sy + SY_ACK, 256, tag, lane); if (lane == 0) sy_st(sy + SY_E, tag); }
        if (w == 11) { w2_stage(0, 0, 1, 30); w2_stage(1, 0, 1, 30); WAIT_VM(30); if (lane == 0) lds_st(lds + AUX_GO + 8, 1u); w2_stage(2, 0, 1, 30); }
    }
    if (w >= 8) {
        const int lw = w - 8;
        if (lw == 3) WAIT_BAR(0); else LBAR();
        STAMP(6);
        if (lw > 0) {
            w2_stage(3, lw - 1, 3, 10);
            if (s_pending >= 0) { WAIT_VM(10); if (lane == 0) sy_st(sy + SY_S + s_pending, tag); }
            w2_stage(4, lw - 1, 3, 10);
            if (G2S == 0) {
                WAIT_BAR(10); w2_stage(5, lw - 1, 3, 10);
                WAIT_BAR(10); w2_stage(6, lw - 1, 3, 10);
                WAIT_BAR(10); w2_stage(7, lw - 1, 3, 10);
                WAIT_BAR(10); w2_stage(8, lw - 1, 3, 10);
                WAIT_BAR(10); WAIT_BAR(0);
            } else if (G2S == 1) {
                WAIT_BAR(10); w2_stage(5, lw - 1, 3, 10); w2_stage(6, lw - 1, 3, 10);
                WAIT_BAR(20); w2_stage(7, lw - 1, 3, 10);
                WAIT_BAR(20); w2_stage(8, lw - 1, 3, 10);
                WAIT_BAR(20); WAIT_BAR(10); WAIT_BAR(0);
            } else {
                w2_stage(5, lw - 1, 3, 10);
                WAIT_BAR(20); w2_stage(6, lw - 1, 3, 10); w2_stage(7, lw - 1, 3, 10);
                WAIT_BAR(30); w2_stage(8, lw - 1, 3, 10);
                WAIT_BAR(30); WAIT_BAR(20); WAIT_BAR(10); WAIT_BAR(0);
            }
        } else {
            const srd_t sx = mk_srd(p.x, L_ * D_ * 2);
            unsigned f0, f1;
            if (s_pending >= 0) publish(sy + SY_S + s_pending, tag, lane);
            LBAR(); LBAR();
            asm volatile("buffer_load_dword %0, %1, %2, %3 offen sc1" : "=v"(f0) : "v"(128u * lane), "s"(sws), "s"(WS_SYNC + 128u * (SY_S + 80 * dt)) : "memory");
            asm volatile("buffer_load_dword %0, %1, %2, %3 offen sc1" : "=v"(f1) : "v"(128u * (64 + (lane & 15))), "s"(sws), "s"(WS_SYNC + 128u * (SY_S + 80 * dt)) : "memory");
            LBAR();
#pragma unroll
            for (int i = 0; i < 10; ++i) { const unsigned o = i * 64 + lane; dma16(lds0 + G2_X + i * 1024, (o / 10) * 1280 + (o % 10) * 16, sx, (unsigned)(rt * 64 * 1280 + dt * 160)); }
            asm volatile("s_waitcnt vmcnt(10)" : "+v"(f0), "+v"(f1) :: "memory");
            const bool s_ready = __all(f0 == tag && f1 == tag);
            auto s_tile = [&]() {
#pragma unroll
                for (int i = 0; i < 10; ++i) dma16_dev(lds0 + G2_S + i * 1024, vl16, sws, WS_ST + (unsigned)(rt * D_ + 80 * dt) * 128 + i * 1024); };
            if (s_ready) s_tile();
            LBAR(); LBAR(); LBAR();
            if (!s_ready) { poll_all<2, 2>(sy + SY_S + 80 * dt, 80, tag, lane); s_tile(); }
            STAMP(7);
            WAIT_VM(0);
        }
        LBAR();
        STAMP(8);
    } else {
        const int tt = w & 3, kh = w >> 2, q = lane >> 4;
        const unsigned soh = WS_H + (unsigned)((rt * 54 + 27 * kh) * 4 + tt) * 1024;
        __builtin_amdgcn_s_setprio(3);
        if (w == 0) { if (gemm_wg) poll_all<1, SL_H>(sy + SY_H + 64 * rt, 54, tag, lane); else poll_all<1, SL_HH>(sy + SY_H + 64 * rt, 54, tag, lane);
                      if (lane == 0) lds_st(lds + AUX_GO, 1u); }
        else lds_wait(lds + AUX_GO, 1, 1, lane);
        STAMP(5);
        G2Regs r;
        lds_ptr pw = lds + kh * G2_HALF + vl16;
#pragma unroll
        for (int k = 0; k < HD; ++k) { hload(r.hb[k], vl16, sws, soh + k * 4096); if (k == 1) __builtin_amdgcn_s_setprio(1); }
        lds_wait(lds + AUX_GO + 8, 1, 1, lane);
#pragma unroll
        for (int j = 0; j < 5; ++j) { r.acc[j] = f32x4{0.f, 0.f, 0.f, 0.f}; r.w[0][j] = *(LAS bf16x8*)(pw + g2_slot(0) + j * 1024); }
        SB();
        g2_steps<0>(r, pw, lane, sws, soh, std::make_integer_sequence<int, 2>{});
        G2STAMP(6);
        g2_steps<2>(r, pw, lane, sws, soh, std::make_integer_sequence<int, 1>{});
        G2STAMP(7);
        g2_steps<3>(r, pw, lane, sws, soh, std::make_integer_sequence<int, 6>{});
        G2STAMP(11);
        g2_steps<9>(r, pw, lane, sws, soh, std::make_integer_sequence<int, 9>{});
        G2STAMP(13);
        g2_steps<18>(r, pw, lane, sws, soh, std::make_integer_sequence<int, 9>{});
        STAMP(8);
        lds_ptr px = lds + g2_slot(5) + tt * 5120 + vl16;
        if (kh) { *(LAS f32x4*)(px) = r.acc[0]; *(LAS f32x4*)(px + 1024) = r.acc[1]; *(LAS f32x4*)(px + 2048) = r.acc[2]; }
        else    { *(LAS f32x4*)(px + 3072) = r.acc[3]; *(LAS f32x4*)(px + 4096) = r.acc[4]; }
        LBAR();
        STAMP(9);
        auto fin = [&](int j, f32x4 a) {
            const int ro = (tt * 16 + (lane & 15)) * 160 + 32 * j + 8 * q;
            const u32x2 xx = *(LAS u32x2*)(lds + G2_X + ro);
            a += *(LAS f32x4*)(px + j * 1024);
            const s16x4 sv = tr_read(lds + G2_S + (16 * j + 4 * q + ((lane & 15) >> 2)) * 128 + tt * 32 + (lane & 3) * 8);
            float o[4];
#pragma unroll
            for (int i = 0; i < 4; ++i) {
                const float sg = a[i] * __builtin_amdgcn_rcpf(1.f + __builtin_amdgcn_exp2f(-1.44269504088896341f * a[i]));
                o[i] = fmaf(bf2f((unsigned short)sv[i]), sg, (i & 1) ? bfhi(xx[i >> 1]) : bflo(xx[i >> 1]));
            }
            *(LAS u32x2*)(lds + G2_O + ro) = u32x2{pk_bf16(o[0], o[1]), pk_bf16(o[2], o[3])};
        };
        if (kh == 0) { fin(0, r.acc[0]); fin(1, r.acc[1]); fin(2, r.acc[2]); } else { fin(3, r.acc[3]); fin(4, r.acc[4]); }
    }
    LBAR();
    if (w < 10) *(u32x4*)(p.out + (size_t)(rt * 64 + tid / 10) * D_ + 80 * dt + (tid % 10) * 8) = *(LAS u32x4*)(lds + G2_O + tid * 16);
    STAMP(10);
    STAMP_FLUSH(p.ws, (gemm_wg ? STWG : STWH));
}

extern "C" void kernel_launch(void* const* d_in, const int* in_sizes, int n_in, void* d_out, int out_size, void* d_ws, size_t ws_size,
                              hipStream_t stream) {
    Params p;
    p.x = (cu16p)d_in[0]; p.lar = (cu16p)d_in[1]; p.aim = (cu16p)d_in[2]; p.cre = (cu16p)d_in[3]; p.cim = (cu16p)d_in[4];
    p.ldt = (cu16p)d_in[5]; p.dsk = (cu16p)d_in[6]; p.wg = (cu16p)d_in[8]; p.wu = (cu16p)d_in[9]; p.wd = (cu16p)d_in[10];
    p.out = (unsigned short*)d_out; p.ws = (char*)d_ws;
    (void)hipFuncSetAttribute((const void*)fused_kernel, hipFuncAttributeMaxDynamicSharedMemorySize, LDS_BYTES);
    fused_kernel<<<256, 768, LDS_BYTES, stream>>>(p);
}
```
